```python
import torch

def _softmax(x, axis=-1):
    return torch.softmax(x, dim=axis)
def _gelu(x):
    return torch.nn.functional.gelu(x, approximate="tanh")
def _silu(x):
    return torch.nn.functional.silu(x)
def _sigmoid(x):
    return torch.sigmoid(x)
def _layernorm(x, axis=-1, eps=1e-5):
    m = x.mean(dim=axis, keepdim=True)
    v = x.var(dim=axis, keepdim=True, unbiased=False)
    return (x - m) / torch.sqrt(v + eps)
def _scatter_add(tgt, idx, src, axis=0):
    return tgt.clone().index_add_(axis, idx, src)
def _segment_sum(src, seg, n):
    out = torch.zeros((n,) + src.shape[1:], dtype=src.dtype)
    return out.index_add_(0, seg, src)
def _pad(x, pw):
    flat = tuple(v for pair in reversed(pw) for v in pair)
    return torch.nn.functional.pad(x, flat)

kernel_name = 'interleave_layers__sliding_local_dilated__grouped_kv_mqa__prompt'

def setup_inputs():

    _w = (lambda a: (lambda t: t.to(torch.bfloat16) if t.is_floating_point() else t)(torch.as_tensor(a)))
    def _A_setup():
        B, S, D = 1, 2048, 576
        return {
            "x": _w((lambda *s: torch.randn(*s, dtype=torch.bfloat16) * 0.05)(B, S, D)),
            "wqkv": _w((lambda *s: torch.randn(*s, dtype=torch.bfloat16) * 0.05)(D, 3 * D)),
            "wo": _w((lambda *s: torch.randn(*s, dtype=torch.bfloat16) * 0.05)(D, D)),
        }
    _A_in = _A_setup()

    def _B_setup():
        B, S, D, H = 1, 2048, 576, 32
        Dh = D // H
        return {
            "x": _w((lambda *s: torch.randn(*s, dtype=torch.bfloat16) * 0.05)(B, S, D)),
            "wq": _w((lambda *s: torch.randn(*s, dtype=torch.bfloat16) * 0.05)(D, H * Dh)),
            "wk": _w((lambda *s: torch.randn(*s, dtype=torch.bfloat16) * 0.05)(D, Dh)),
            "wv": _w((lambda *s: torch.randn(*s, dtype=torch.bfloat16) * 0.05)(D, Dh)),
            "wo": _w((lambda *s: torch.randn(*s, dtype=torch.bfloat16) * 0.05)(H * Dh, D)),
        }
    _B_in = _B_setup()

    return {
        "A_x": _A_in["x"],
        "A_wqkv": _A_in["wqkv"],
        "A_wo": _A_in["wo"],
        "B_x": _B_in["x"],
        "B_wq": _B_in["wq"],
        "B_wk": _B_in["wk"],
        "B_wv": _B_in["wv"],
        "B_wo": _B_in["wo"],
    }

def reference(A_x, A_wqkv, A_wo, B_x, B_wq, B_wk, B_wv, B_wo):
    def _A_ref(x, wqkv, wo):
        B, S, D = x.shape
        H = 32; Dh = D // H
        W, dil = 8, 2
        qkv = (lambda x, *d: x.permute(*d))((x @ wqkv).reshape(B, S, 3, H, Dh), 2, 0, 3, 1, 4)
        q, k, v = qkv[0], qkv[1], qkv[2]
        scale = Dh ** -0.5
        pos = torch.arange(S)
        off = torch.arange(W) * dil
        idx = pos[:, None] - off[None, :]
        mask = idx >= 0
        idx_c = torch.where(mask, idx, 0).reshape(-1)
        kg = k[:, :, idx_c, :].reshape(B, H, S, W, Dh)
        vg = v[:, :, idx_c, :].reshape(B, H, S, W, Dh)
        logits = torch.einsum("bhsd,bhswd->bhsw", q, kg) * scale
        logits = torch.where(mask[None, None, :, :], logits, -1e9)
        attn = _softmax(logits, axis=-1)
        o = torch.einsum("bhsw,bhswd->bhsd", attn, vg)
        return (lambda x, *d: x.permute(*d))(o, 0, 2, 1, 3).reshape(B, S, D) @ wo

    def _B_ref(x, wq, wk, wv, wo):
        B, S, D = x.shape
        H = 32
        Dh = D // H
        q = (lambda x, *d: x.permute(*d))((x @ wq).reshape(B, S, H, Dh), 0, 2, 1, 3)
        k = x @ wk
        v = x @ wv
        scores = torch.einsum("bhqd,bkd->bhqk", q, k) * (Dh ** -0.5)
        p = _softmax(scores, axis=-1)
        o = torch.einsum("bhqk,bkd->bhqd", p, v)
        return (lambda x, *d: x.permute(*d))(o, 0, 2, 1, 3).reshape(B, S, H * Dh) @ wo

    x = A_x
    x = x + _A_ref(_layernorm(x, axis=-1), A_wqkv, A_wo)
    x = x + _B_ref(_layernorm(x, axis=-1), B_wq, B_wk, B_wv, B_wo)
    x = x + _A_ref(_layernorm(x, axis=-1), A_wqkv, A_wo)
    x = x + _B_ref(_layernorm(x, axis=-1), B_wq, B_wk, B_wv, B_wo)
    return x
```

```cpp
#include <hip/hip_runtime.h>
#include <hip/hip_bf16.h>
#include <stdint.h>

#define LAS __attribute__((address_space(3)))
#define GAS __attribute__((address_space(1)))
#define DI __device__ __forceinline__

typedef __attribute__((ext_vector_type(8))) __bf16 bf16x8;
typedef __attribute__((ext_vector_type(2))) __bf16 bf16x2;
typedef __attribute__((ext_vector_type(4))) short s16x4;
typedef __attribute__((ext_vector_type(8))) short s16x8;
typedef __attribute__((ext_vector_type(2))) float f32x2;
typedef __attribute__((ext_vector_type(4))) float f32x4;
typedef __attribute__((ext_vector_type(16))) float f32x16;
typedef __attribute__((ext_vector_type(2))) unsigned u32x2;
typedef __attribute__((ext_vector_type(4))) unsigned u32x4;

constexpr int SEQ = 2048;
constexpr int DM = 576;
constexpr float LN_EPS = 1e-5f;
constexpr float QSCALE = 0.23570226039551584f * 1.4426950408889634f;

constexpr int KV_TILE = 2368;
constexpr int KV_KX = 1024;
constexpr int KV_V = 1152;
constexpr int KV_VG = 608;
constexpr int KV_VH = 304;
constexpr int KV_BYTES = 64 * KV_TILE;

constexpr size_t ACT = (size_t)SEQ * DM * 2;
constexpr size_t WS_OA = 0;
constexpr size_t WS_X1 = ACT;
constexpr size_t WS_X2 = 2 * ACT;
constexpr size_t WS_X3 = 3 * ACT;
constexpr size_t WS_Q = 4 * ACT;
constexpr size_t WS_OB = 5 * ACT;
constexpr size_t WS_KV = 6 * ACT;
constexpr size_t WS_CS = WS_KV + 262144;

DI unsigned lds_addr(const void* p) { return (unsigned)(uintptr_t)(LAS const char*)p; }

DI void glds16(const void* base, unsigned voff, unsigned lds_dst) {
  unsigned keep;
  asm volatile("s_mov_b32 %0, m0\n\ts_mov_b32 m0, %3\n\ts_nop 0\n\tglobal_load_lds_dwordx4 %1, %2\n\ts_mov_b32 m0, %0"
               : "=&s"(keep) : "v"(voff), "s"(base), "s"(lds_dst) : "memory");
}
DI void glds16v(const void* gsrc, unsigned lds_dst) {
  unsigned keep;
  asm volatile("s_mov_b32 %0, m0\n\ts_mov_b32 m0, %2\n\ts_nop 0\n\tglobal_load_lds_dwordx4 %1, off\n\ts_mov_b32 m0, %0"
               : "=&s"(keep) : "v"(gsrc), "s"(lds_dst) : "memory");
}
DI void glds4v(const void* gsrc, unsigned lds_dst) {
  unsigned keep;
  asm volatile("s_mov_b32 %0, m0\n\ts_mov_b32 m0, %2\n\ts_nop 0\n\tglobal_load_lds_dword %1, off\n\ts_mov_b32 m0, %0"
               : "=&s"(keep) : "v"(gsrc), "s"(lds_dst) : "memory");
}
template <int N> DI void wait_vm() { asm volatile("s_waitcnt vmcnt(%0)" ::"i"(N) : "memory"); }
DI void bar() { asm volatile("s_barrier" ::: "memory"); }
DI void bar_lgkm() { asm volatile("s_waitcnt lgkmcnt(0)\n\ts_barrier" ::: "memory"); }

DI s16x4 tr_read(unsigned a) {
  return __builtin_amdgcn_ds_read_tr16_b64_v4i16((LAS s16x4*)(uintptr_t)a);
}
DI bf16x8 tr_frag(unsigned a0, unsigned a1) {
  s16x4 lo = tr_read(a0), hi = tr_read(a1);
  s16x8 v = {lo[0], lo[1], lo[2], lo[3], hi[0], hi[1], hi[2], hi[3]};
  return __builtin_bit_cast(bf16x8, v);
}
template <class T> DI T lds_ld(unsigned a) { return *(LAS const T*)(uintptr_t)a; }
template <class T> DI void lds_st(unsigned a, T v) { *(LAS T*)(uintptr_t)a = v; }

DI unsigned pk_bf16(float a, float b) {
  f32x2 v = {a, b};
  return __builtin_bit_cast(unsigned, __builtin_convertvector(v, bf16x2));
}
DI float dot2(unsigned a, unsigned b, float c) {
  return __builtin_amdgcn_fdot2_f32_bf16(__builtin_bit_cast(bf16x2, a), __builtin_bit_cast(bf16x2, b), c, false);
}
DI float bf_lo(unsigned u) { return __uint_as_float(u << 16); }
DI float bf_hi(unsigned u) { return __uint_as_float(u & 0xffff0000u); }
DI f32x4 mfma16(bf16x8 a, bf16x8 b, f32x4 c) { return __builtin_amdgcn_mfma_f32_16x16x32_bf16(a, b, c, 0, 0, 0); }
DI f32x16 mfma32(bf16x8 a, bf16x8 b, f32x16 c) { return __builtin_amdgcn_mfma_f32_32x32x16_bf16(a, b, c, 0, 0, 0); }
DI f32x16 mfma32x8(u32x2 a, u32x2 b, f32x16 c) {
  return __builtin_amdgcn_mfma_f32_32x32x8bf16_1k(__builtin_bit_cast(s16x4, a), __builtin_bit_cast(s16x4, b), c, 0, 0, 0);
}
DI int phys2log(int r) { return (r & ~7) + ((r & 1) << 2) + ((r >> 1) & 3); }

DI float gram_diag(f32x4 G, int lane) {
  int c = lane & 15;
  float v = (c & 2) ? ((c & 1) ? G[3] : G[2]) : ((c & 1) ? G[1] : G[0]);
  return __shfl(v, ((c >> 2) << 4) + c, 64);
}

constexpr int WO_RS = 144;
constexpr int WO_LDS = DM * WO_RS + 64;

DI void load_w72(const void* W, int cb, int lw, int lane, unsigned lds0) {
  unsigned off[3];
#pragma unroll
  for (int i = 0; i < 3; i++) {
    int pi = (lw * 3 + i) * 64 + lane;
    int rl = pi / 9, pc = pi - rl * 9;
    off[i] = phys2log(rl) * (DM * 2) + cb * 144 + pc * 16;
  }
#pragma unroll
  for (int c = 0; c < 9; c++)
#pragma unroll
    for (int i = 0; i < 3; i++) glds16(W, off[i] + c * 64 * DM * 2, lds0 + c * 9216 + (lw * 3 + i) * 1024);
}
DI void loader_waits27() {
  wait_vm<24>(); bar(); wait_vm<21>(); bar(); wait_vm<18>(); bar(); wait_vm<15>(); bar(); wait_vm<12>(); bar();
  wait_vm<9>(); bar(); wait_vm<6>(); bar(); wait_vm<3>(); bar(); wait_vm<0>(); bar();
}

__global__ void __launch_bounds__(448) k_wo(const uint16_t* __restrict__ o, const uint16_t* __restrict__ W,
                                            const uint16_t* __restrict__ xin, uint16_t* __restrict__ xout) {
  extern __shared__ __attribute__((aligned(16))) char smem[];
  const unsigned lds0 = lds_addr(smem);
  const int b = blockIdx.x;
  const int mt = (b & 7) * 4 + (b >> 6), cb = (b >> 3) & 7;
  const int lane = threadIdx.x & 63;
  const int wave = __builtin_amdgcn_readfirstlane(threadIdx.x >> 6);
  if (wave >= 4) {
    load_w72(W, cb, wave - 4, lane, lds0);
    loader_waits27();
    return;
  }
  const int c = lane & 15, g = lane >> 4;
  const int row = mt * 64 + wave * 16 + c;
  bf16x8 xf[18];
#pragma unroll
  for (int s = 0; s < 18; s++) xf[s] = *(const bf16x8*)(o + row * DM + s * 32 + g * 8);
  u32x2 res[5];
#pragma unroll
  for (int t = 0; t < 5; t++) {
    res[t] = u32x2{0, 0};
    if (t < 4 || g < 2) res[t] = *(const u32x2*)(xin + row * DM + cb * 72 + t * 16 + g * 4);
  }
  f32x4 acc[5];
#pragma unroll
  for (int t = 0; t < 5; t++) acc[t] = f32x4{0, 0, 0, 0};
  const unsigned abase = lds0 + (8 * g + 2 * (c >> 2)) * WO_RS + 8 * (c & 3);
#pragma unroll
  for (int s = 0; s < 18; s++) {
    if ((s & 1) == 0) bar();
#pragma unroll
    for (int t = 0; t < 5; t++) {
      unsigned a = abase + s * 32 * WO_RS + t * 32;
      acc[t] = mfma16(tr_frag(a, a + WO_RS), xf[s], acc[t]);
    }
  }
#pragma unroll
  for (int t = 0; t < 5; t++) {
    if (t < 4 || g < 2) {
      u32x2 r;
      r[0] = pk_bf16(acc[t][0] + bf_lo(res[t][0]), acc[t][1] + bf_hi(res[t][0]));
      r[1] = pk_bf16(acc[t][2] + bf_lo(res[t][1]), acc[t][3] + bf_hi(res[t][1]));
      *(u32x2*)(xout + row * DM + cb * 72 + t * 16 + g * 4) = r;
    }
  }
}

constexpr int PJ_EX = DM * WO_RS;
constexpr int PJ_LDS = PJ_EX + DM * 16 + 64;

DI int kv_pair(int cb, int i) { return i < 2 ? 2 * cb + i : 16 + cb; }

__global__ void __launch_bounds__(512) k_proj(const uint16_t* __restrict__ x, const uint16_t* __restrict__ wq,
                                              const uint16_t* __restrict__ wk, const uint16_t* __restrict__ wv,
                                              const float* __restrict__ cs2, uint16_t* __restrict__ q,
                                              char* __restrict__ kv) {
  extern __shared__ __attribute__((aligned(16))) char smem[];
  const unsigned lds0 = lds_addr(smem);
  const int b = blockIdx.x;
  const int mt = (b & 7) * 4 + (b >> 6), cb = (b >> 3) & 7;
  const int lane = threadIdx.x & 63;
  const int wave = __builtin_amdgcn_readfirstlane(threadIdx.x >> 6);
  if (wave >= 4) {
    if (wave < 7) {
      load_w72(wq, cb, wave - 4, lane, lds0);
      loader_waits27();
    } else {
      const int dw = lane & 3;
      const bool dma = dw < 2 || (dw == 2 && cb < 2);
      if (!dma) {
        unsigned v = dw == 3 ? 0x00003F80u : 0u;
#pragma unroll
        for (int i = 0; i < 36; i++) lds_st<unsigned>(lds0 + PJ_EX + i * 256 + lane * 4, v);
      }
      const int P = kv_pair(cb, dw < 3 ? dw : 0);
      const uint16_t* src = (P < 9 ? wk + 2 * P : wv + 2 * (P - 9));
      if (dma) {
#pragma unroll
        for (int i = 0; i < 36; i++)
          glds4v(src + phys2log(i * 16 + (lane >> 2)) * 18, lds0 + PJ_EX + i * 256);
      }
      asm volatile("s_waitcnt lgkmcnt(0)" ::: "memory");
      wait_vm<32>(); bar(); wait_vm<28>(); bar(); wait_vm<24>(); bar(); wait_vm<20>(); bar(); wait_vm<16>(); bar();
      wait_vm<12>(); bar(); wait_vm<8>(); bar(); wait_vm<4>(); bar(); wait_vm<0>(); bar();
    }
    return;
  }
  const int c = lane & 15, g = lane >> 4;
  const int row = mt * 64 + wave * 16 + c;
  bf16x8 xf[18];
#pragma unroll
  for (int s = 0; s < 18; s++) xf[s] = *(const bf16x8*)(x + row * DM + s * 32 + g * 8);
  f32x4 cs[5];
#pragma unroll
  for (int t = 0; t < 5; t++) cs[t] = *(const f32x4*)(cs2 + cb * 80 + t * 16 + g * 4);
  f32x4 acc[5], G = {0, 0, 0, 0};
#pragma unroll
  for (int t = 0; t < 5; t++) acc[t] = f32x4{0, 0, 0, 0};
  const int qq = c >> 2, p = c & 3;
  const unsigned abase = lds0 + (8 * g + 2 * qq) * WO_RS + 8 * p;
  const unsigned a4base = p < 2 ? abase + 128 : lds0 + PJ_EX + (8 * g + 2 * qq) * 16 + 8 * (p - 2);
  const unsigned a4step = p < 2 ? 32 * WO_RS : 32 * 16;
  const unsigned a4e = p < 2 ? WO_RS : 16;
#pragma unroll
  for (int s = 0; s < 18; s++) {
    if ((s & 1) == 0) bar();
#pragma unroll
    for (int t = 0; t < 4; t++) {
      unsigned a = abase + s * 32 * WO_RS + t * 32;
      acc[t] = mfma16(tr_frag(a, a + WO_RS), xf[s], acc[t]);
    }
    unsigned a4 = a4base + s * a4step;
    acc[4] = mfma16(tr_frag(a4, a4 + a4e), xf[s], acc[4]);
    G = mfma16(xf[s], xf[s], G);
  }
  const float sx = __shfl(acc[4][2], 48 + c, 64);
  const float sxx = gram_diag(G, lane);
  const float mean = sx * (1.0f / DM);
  const float var = fmaxf(sxx * (1.0f / DM) - mean * mean, 0.f);
  const float rstd = rsqrtf(var + LN_EPS);
#pragma unroll
  for (int t = 0; t < 5; t++) {
    f32x4 y;
#pragma unroll
    for (int r = 0; r < 4; r++) y[r] = (acc[t][r] - mean * cs[t][r]) * rstd;
    if (t < 4 || g < 2) {
      u32x2 r;
      r[0] = pk_bf16(y[0] * QSCALE, y[1] * QSCALE);
      r[1] = pk_bf16(y[2] * QSCALE, y[3] * QSCALE);
      *(u32x2*)(q + row * DM + cb * 72 + t * 16 + g * 4) = r;
    } else {
      const int tile = row >> 5, r32 = row & 31;
      char* tb = kv + tile * KV_TILE;
#pragma unroll
      for (int j = 0; j < 2; j++) {
        const int i = (g - 2) * 2 + j;
        if (i < 2 || (i == 2 && cb < 2)) {
          const int P = kv_pair(cb, i);
          const unsigned u = pk_bf16(y[2 * j], y[2 * j + 1]);
          if (P < 9) {
            const int d = 2 * P;
            if (d < 16) *(unsigned*)(tb + (d >> 3) * 512 + r32 * 16 + (d & 7) * 2) = u;
            else *(unsigned*)(tb + KV_KX + r32 * 4) = u;
          } else {
            const int d = 2 * (P - 9);
            const int o16 = r32 & 15;
            const int hh = (o16 >> 2) & 1, jj = (o16 & 3) + 4 * (o16 >> 3);
            char* vb = tb + KV_V + (r32 >> 4) * KV_VG + hh * KV_VH + jj * 2;
            *(uint16_t*)(vb + d * 16) = (uint16_t)(u & 0xffff);
            *(uint16_t*)(vb + (d + 1) * 16) = (uint16_t)(u >> 16);
          }
        } else if (i == 3 && cb == 7) {
          const int o16 = r32 & 15;
          const int hh = (o16 >> 2) & 1, jj = (o16 & 3) + 4 * (o16 >> 3);
          *(uint16_t*)(tb + KV_V + (r32 >> 4) * KV_VG + hh * KV_VH + jj * 2 + 18 * 16) = 0x3F80;
        }
      }
    }
  }
}

constexpr int AT_Q = KV_BYTES;
constexpr int AT_FLAG = AT_Q + 9216;
constexpr int AT_LDS = AT_FLAG + 64;

struct AttnAddr { unsigned k, kx, v; };

template <bool SHIFT>
DI void attn_run(AttnAddr a, int ntiles, bf16x8 qf, u32x2 qx, f32x16& O, f32x16 negm) {
#pragma unroll 2
  for (int i = 0; i < ntiles; i++) {
    const unsigned off = i * (2 * KV_TILE);
    const bf16x8 kf = lds_ld<bf16x8>(a.k + off);
    const u32x2 kx = {lds_ld<unsigned>(a.kx + off), 0u};
    const bf16x8 v0 = lds_ld<bf16x8>(a.v + off);
    const bf16x8 v1 = lds_ld<bf16x8>(a.v + off + KV_VG);
    f32x16 s;
    if (SHIFT) s = mfma32(kf, qf, negm);
    else s = mfma32(kf, qf, f32x16{0, 0, 0, 0, 0, 0, 0, 0, 0, 0, 0, 0, 0, 0, 0, 0});
    s = mfma32x8(kx, qx, s);
    u32x4 p0, p1;
#pragma unroll
    for (int j = 0; j < 4; j++) {
      p0[j] = pk_bf16(__builtin_amdgcn_exp2f(s[2 * j]), __builtin_amdgcn_exp2f(s[2 * j + 1]));
      p1[j] = pk_bf16(__builtin_amdgcn_exp2f(s[8 + 2 * j]), __builtin_amdgcn_exp2f(s[8 + 2 * j + 1]));
    }
    O = mfma32(v0, __builtin_bit_cast(bf16x8, p0), O);
    O = mfma32(v1, __builtin_bit_cast(bf16x8, p1), O);
  }
}

__global__ void __launch_bounds__(1024) k_attn(const uint16_t* __restrict__ q, const char* __restrict__ kv,
                                               uint16_t* __restrict__ o) {
  extern __shared__ __attribute__((aligned(16))) char smem[];
  const unsigned lds0 = lds_addr(smem);
  const int b = blockIdx.x;
  const int mt = (b & 7) * 4 + (b >> 6), cb = (b >> 3) & 7;
  const int lane = threadIdx.x & 63;
  const int wave = __builtin_amdgcn_readfirstlane(threadIdx.x >> 6);
  if (wave < 9) {
    const int pi = wave * 64 + lane;
    const int tk = pi / 9, pc = pi - tk * 9;
    glds16(q, (mt * 64 + tk) * (DM * 2) + cb * 144 + pc * 16, lds0 + AT_Q + wave * 1024);
  }
  if (wave < 4) glds16(kv, wave * 1024 + lane * 16, lds0 + wave * 1024);
#pragma unroll
  for (int i = 0; i < 9; i++) {
    const int ii = 4 + i * 16 + wave;
    glds16(kv, ii * 1024 + lane * 16, lds0 + ii * 1024);
  }
  if (threadIdx.x == 0) lds_st<unsigned>(lds0 + AT_FLAG, 0u);
  const int qt = wave & 7, kh = wave >> 3;
  const int th = qt & 1, hh = qt >> 1;
  const int r = lane & 31, h = lane >> 5;
  wait_vm<7>();
  bar_lgkm();
  const unsigned qa = lds0 + AT_Q + (th * 32 + r) * 144 + hh * 36;
  u32x4 qw;
#pragma unroll
  for (int j = 0; j < 4; j++) qw[j] = lds_ld<unsigned>(qa + h * 16 + j * 4);
  const bf16x8 qf = __builtin_bit_cast(bf16x8, qw);
  const u32x2 qx = {h == 0 ? lds_ld<unsigned>(qa + 32) : 0u, 0u};
  AttnAddr a;
  a.k = lds0 + kh * KV_TILE + h * 512 + r * 16;
  a.kx = lds0 + kh * KV_TILE + KV_KX + r * 4;
  a.v = lds0 + kh * KV_TILE + KV_V + h * KV_VH + (r < 18 ? r : 18) * 16;
  f32x16 O = {0, 0, 0, 0, 0, 0, 0, 0, 0, 0, 0, 0, 0, 0, 0, 0};
  attn_run<false>(a, 7, qf, qx, O, O);
  wait_vm<0>();
  bar();
  {
    AttnAddr a2 = {a.k + 7 * 2 * KV_TILE, a.kx + 7 * 2 * KV_TILE, a.v + 7 * 2 * KV_TILE};
    attn_run<false>(a2, 25, qf, qx, O, O);
  }
  float l = h == 0 ? O[10] : O[8];
  {
    float mx = 0.f;
#pragma unroll
    for (int j = 0; j < 10; j++) mx = fmaxf(mx, fabsf(O[j]));
    bool ok = (l >= 8.2718061e-25f) && (l <= 1.2676506e30f) && (mx <= 3.0e38f);
#pragma unroll
    for (int j = 0; j < 10; j++) ok = ok && (O[j] == O[j]);
    if (__ballot(!ok) != 0ull && lane == 0) lds_st<unsigned>(lds0 + AT_FLAG, 1u);
  }
  bar_lgkm();
  if (lds_ld<unsigned>(lds0 + AT_FLAG) != 0u) {
    AttnAddr f;
    f.k = lds0 + h * 512 + r * 16;
    f.kx = lds0 + KV_KX + r * 4;
    float m = -3.0e38f;
    for (int i = 0; i < 64; i++) {
      const bf16x8 kf = lds_ld<bf16x8>(f.k + i * KV_TILE);
      const u32x2 kx = {lds_ld<unsigned>(f.kx + i * KV_TILE), 0u};
      f32x16 s = mfma32(kf, qf, f32x16{0, 0, 0, 0, 0, 0, 0, 0, 0, 0, 0, 0, 0, 0, 0, 0});
      s = mfma32x8(kx, qx, s);
#pragma unroll
      for (int j = 0; j < 16; j++) m = fmaxf(m, s[j]);
    }
    m = fmaxf(m, __shfl_xor(m, 32, 64));
    f32x16 negm;
#pragma unroll
    for (int j = 0; j < 16; j++) { negm[j] = -m; O[j] = 0.f; }
    attn_run<true>(a, 32, qf, qx, O, negm);
  }
  bar();
  const unsigned mb = lds0 + qt * (11 * 256) + lane * 4;
  if (kh == 1) {
#pragma unroll
    for (int j = 0; j < 11; j++) lds_st<float>(mb + j * 256, O[j]);
  }
  bar_lgkm();
  if (kh == 0) {
#pragma unroll
    for (int j = 0; j < 11; j++) O[j] += lds_ld<float>(mb + j * 256);
    l = h == 0 ? O[10] : O[8];
    const float inv = 1.0f / l;
    uint16_t* dst = o + (mt * 64 + th * 32 + r) * DM + (cb * 4 + hh) * 18 + h * 4;
    u32x2 w0 = {pk_bf16(O[0] * inv, O[1] * inv), pk_bf16(O[2] * inv, O[3] * inv)};
    u32x2 w1 = {pk_bf16(O[4] * inv, O[5] * inv), pk_bf16(O[6] * inv, O[7] * inv)};
    *(u32x2*)(dst) = w0;
    *(u32x2*)(dst + 8) = w1;
    if (h == 0) *(unsigned*)(dst + 16) = pk_bf16(O[8] * inv, O[9] * inv);
  }
}

constexpr int A_RS = 240;
constexpr int A_HALO = DM * A_RS;
constexpr int A_ONES = A_HALO + 18 * 1024;
constexpr int A_CS = A_ONES + 16;
constexpr int A_HST = A_CS + 112 * 4;
constexpr int A_LDS = A_HST + 16 * 8;
constexpr int A_QK = 0, A_QKS = 208;
constexpr int A_V = 144 * A_QKS, A_VS = 176;

__global__ void __launch_bounds__(512) k_local(const uint16_t* __restrict__ x, const uint16_t* __restrict__ W,
                                               uint16_t* __restrict__ o, const uint16_t* __restrict__ wq,
                                               const uint16_t* __restrict__ wk, const uint16_t* __restrict__ wv,
                                               float* __restrict__ cs2, int do_cs) {
  extern __shared__ __attribute__((aligned(16))) char smem[];
  const unsigned lds0 = lds_addr(smem);
  const int b = blockIdx.x;
  const int mt = (b & 7) * 2 + (b >> 7), hp = (b >> 3) & 15;
  const int s0 = mt * 128;
  const int lane = threadIdx.x & 63;
  const int wave = __builtin_amdgcn_readfirstlane(threadIdx.x >> 6);
  const int c = lane & 15, g = lane >> 4;
  if (wave >= 4) {
    if (wave < 7) {
      const int lw = wave - 4;
      unsigned off[5];
#pragma unroll
      for (int i = 0; i < 5; i++) {
        int pi = (lw * 5 + i) * 64 + lane;
        int rl = pi / 15, pc15 = pi - rl * 15;
        int sg = pc15 / 5, pc = pc15 - sg * 5;
        off[i] = phys2log(rl) * (3 * DM * 2) + (sg * DM + hp * 36 + (pc < 4 ? 8 * pc : 28)) * 2;
      }
#pragma unroll
      for (int ch = 0; ch < 9; ch++)
#pragma unroll
        for (int i = 0; i < 5; i++)
          glds16(W, off[i] + ch * 64 * 3 * DM * 2, lds0 + ch * 15360 + (lw * 5 + i) * 1024);
      wait_vm<40>(); bar(); wait_vm<35>(); bar(); wait_vm<30>(); bar(); wait_vm<25>(); bar(); wait_vm<20>(); bar();
      wait_vm<15>(); bar(); wait_vm<10>(); bar(); wait_vm<5>(); bar(); wait_vm<0>(); bar();
    } else {
      if (c == 0) {
#pragma unroll
        for (int s = 0; s < 18; s++)
          lds_st<u32x4>(lds0 + A_HALO + s * 1024 + lane * 16, u32x4{0x3F803F80u, 0x3F803F80u, 0x3F803F80u, 0x3F803F80u});
      }
      if (lane == 0) lds_st<u32x2>(lds0 + A_ONES, u32x2{0x00003F80u, 0u});
      if (c != 0) {
        int tok = s0 - 16 + c;
        tok = tok < 0 ? 0 : tok;
#pragma unroll
        for (int s = 0; s < 18; s++) glds16(x, tok * (DM * 2) + s * 64 + g * 16, lds0 + A_HALO + s * 1024);
      }
      asm volatile("s_waitcnt lgkmcnt(0)" ::: "memory");
      wait_vm<16>(); bar(); wait_vm<14>(); bar(); wait_vm<12>(); bar(); wait_vm<10>(); bar(); wait_vm<8>(); bar();
      wait_vm<6>(); bar(); wait_vm<4>(); bar(); wait_vm<2>(); bar(); wait_vm<0>(); bar();
      if (do_cs && b < 160) {
        const int j = b / 20, n0 = (b - j * 20) * 4;
        float sum[4] = {0, 0, 0, 0};
#pragma unroll
        for (int e = 0; e < 4; e++) {
          const int n = n0 + e;
          const uint16_t* src = nullptr;
          int ld = 0;
          if (n < 72) { src = wq + j * 72 + n; ld = DM; }
          else {
            const int i = (n - 72) >> 1;
            if (i < 2 || (i == 2 && j < 2)) {
              const int P = kv_pair(j, i);
              src = (P < 9 ? wk + 2 * P : wv + 2 * (P - 9)) + (n & 1);
              ld = 18;
            }
          }
          if (src) {
#pragma unroll
            for (int i = 0; i < 9; i++) sum[e] += __uint_as_float((unsigned)src[(i * 64 + lane) * ld] << 16);
          }
        }
#pragma unroll
        for (int e = 0; e < 4; e++)
          for (int m = 32; m >= 1; m >>= 1) sum[e] += __shfl_xor(sum[e], m, 64);
        if (lane == 0) *(f32x4*)(cs2 + j * 80 + n0) = f32x4{sum[0], sum[1], sum[2], sum[3]};
      }
    }
    bar();
    bar();
    return;
  }
  bf16x8 xf[2][18];
  const uint16_t* xrow = x + (s0 + wave * 32 + c) * DM + g * 8;
#pragma unroll
  for (int s = 0; s < 12; s++)
#pragma unroll
    for (int tt = 0; tt < 2; tt++) xf[tt][s] = *(const bf16x8*)(xrow + tt * 16 * DM + s * 32);
  const int qq = c >> 2, p = c & 3;
  unsigned ab[7];
#pragma unroll
  for (int t = 0; t < 7; t++) {
    const int G = 4 * t + p;
    const int sg = G / 9, i = G - sg * 9;
    ab[t] = lds0 + (8 * g + 2 * qq) * A_RS + sg * 80 + (i < 8 ? 8 * i : 72);
  }
  const bool ones_lane = (p == 3);
  const unsigned a6step = ones_lane ? 0 : 32 * A_RS;
  const unsigned a6e = ones_lane ? 0 : A_RS;
  if (ones_lane) ab[6] = lds0 + A_ONES;
  unsigned abh0, abh1;
  {
    const int G0 = 8 * wave + p, G1 = G0 + 4;
    const int sg0 = G0 / 9, i0 = G0 - sg0 * 9, sg1 = G1 / 9, i1 = G1 - sg1 * 9;
    abh0 = lds0 + (8 * g + 2 * qq) * A_RS + sg0 * 80 + (i0 < 8 ? 8 * i0 : 72);
    abh1 = lds0 + (8 * g + 2 * qq) * A_RS + sg1 * 80 + (i1 < 8 ? 8 * i1 : 72);
    if (wave == 3) { abh0 = ab[6]; abh1 = ab[5]; }
  }
  const unsigned h0step = wave == 3 ? a6step : 32 * A_RS;
  const unsigned h0e = wave == 3 ? a6e : A_RS;
  f32x4 acc[2][7], G[2], hacc[2];
#pragma unroll
  for (int t = 0; t < 7; t++) { acc[0][t] = f32x4{0, 0, 0, 0}; acc[1][t] = f32x4{0, 0, 0, 0}; }
  G[0] = G[1] = hacc[0] = hacc[1] = f32x4{0, 0, 0, 0};
#pragma unroll
  for (int s = 0; s < 18; s++) {
    if ((s & 1) == 0) bar();
    if (s == 6) {
#pragma unroll
      for (int s2 = 12; s2 < 18; s2++)
#pragma unroll
        for (int tt = 0; tt < 2; tt++) xf[tt][s2] = *(const bf16x8*)(xrow + tt * 16 * DM + s2 * 32);
    }
    const bf16x8 hf = lds_ld<bf16x8>(lds0 + A_HALO + s * 1024 + lane * 16);
#pragma unroll
    for (int t = 0; t < 7; t++) {
      bf16x8 wf;
      if (t < 6) { unsigned a = ab[t] + s * 32 * A_RS; wf = tr_frag(a, a + A_RS); }
      else { unsigned a = ab[6] + s * a6step; wf = tr_frag(a, a + a6e); }
      acc[0][t] = mfma16(wf, xf[0][s], acc[0][t]);
      acc[1][t] = mfma16(wf, xf[1][s], acc[1][t]);
    }
    {
      const unsigned a0 = abh0 + s * h0step, a1 = abh1 + s * 32 * A_RS;
      hacc[0] = mfma16(tr_frag(a0, a0 + h0e), hf, hacc[0]);
      bf16x8 w1 = tr_frag(a1, a1 + A_RS);
      if (wave == 3) w1 = hf;
      hacc[1] = mfma16(w1, hf, hacc[1]);
    }
    G[0] = mfma16(xf[0][s], xf[0][s], G[0]);
    G[1] = mfma16(xf[1][s], xf[1][s], G[1]);
  }
  float mean[2], rstd[2];
#pragma unroll
  for (int tt = 0; tt < 2; tt++) {
    const float sx = __shfl(acc[tt][6][0], 48 + c, 64);
    const float sxx = gram_diag(G[tt], lane);
    mean[tt] = sx * (1.0f / DM);
    rstd[tt] = rsqrtf(fmaxf(sxx * (1.0f / DM) - mean[tt] * mean[tt], 0.f) + LN_EPS);
  }
  if (c == 0) {
    lds_st<f32x4>(lds0 + A_CS + (wave * 32 + g * 4) * 4, hacc[0]);
    if (wave < 3) lds_st<f32x4>(lds0 + A_CS + (wave * 32 + 16 + g * 4) * 4, hacc[1]);
  }
  if (wave == 3) {
    const float sx = __shfl(hacc[0][0], 48 + c, 64);
    const float sxx = gram_diag(hacc[1], lane);
    const float hm = sx * (1.0f / DM);
    const float hr = rsqrtf(fmaxf(sxx * (1.0f / DM) - hm * hm, 0.f) + LN_EPS);
    if (g == 0) lds_st<f32x2>(lds0 + A_HST + c * 8, f32x2{hm, hr});
  }
  bar_lgkm();
  {
    const f32x2 hst = lds_ld<f32x2>(lds0 + A_HST + c * 8);
#pragma unroll
    for (int t = 0; t < 7; t++) {
      const f32x4 cs = lds_ld<f32x4>(lds0 + A_CS + (t * 16 + g * 4) * 4);
#pragma unroll
      for (int j = 0; j < 2; j++) {
        const int n = 16 * t + 4 * g + 2 * j;
        const int grp = (n * 57) >> 10;
        if (n < 108) {
#pragma unroll
          for (int tt = 0; tt < 3; tt++) {
            if (tt == 2 && (t >> 1) != wave) continue;
            const f32x4 av = tt < 2 ? acc[tt][t] : hacc[t & 1];
            const float mu = tt < 2 ? mean[tt] : hst[0], rs = tt < 2 ? rstd[tt] : hst[1];
            const int ts = tt < 2 ? 16 + wave * 32 + tt * 16 + c : c;
            const float y0 = (av[2 * j] - mu * cs[2 * j]) * rs, y1 = (av[2 * j + 1] - mu * cs[2 * j + 1]) * rs;
            if (n < 72) lds_st<unsigned>(lds0 + A_QK + ts * A_QKS + 2 * n + 12 * grp, pk_bf16(y0, y1));
            else lds_st<f32x2>(lds0 + A_V + ts * A_VS + 4 * (n - 72) + 8 * (grp - 4), f32x2{y0, y1});
          }
        }
      }
    }
  }
  bar_lgkm();
  {
    const int h = lane >> 5, tl = wave * 32 + (lane & 31);
    const int ts = 16 + tl;
    unsigned qv[9];
    {
      const unsigned qa = lds0 + A_QK + ts * A_QKS + h * 48;
      u32x4 a = lds_ld<u32x4>(qa), bq = lds_ld<u32x4>(qa + 16);
      qv[0] = a[0]; qv[1] = a[1]; qv[2] = a[2]; qv[3] = a[3];
      qv[4] = bq[0]; qv[5] = bq[1]; qv[6] = bq[2]; qv[7] = bq[3];
      qv[8] = lds_ld<unsigned>(qa + 32);
    }
    float lg[8];
    float m = -3.0e38f;
#pragma unroll
    for (int w = 0; w < 8; w++) {
      const unsigned ka = lds0 + A_QK + (ts - 2 * w) * A_QKS + 96 + h * 48;
      u32x4 a = lds_ld<u32x4>(ka), bk = lds_ld<u32x4>(ka + 16);
      unsigned k8 = lds_ld<unsigned>(ka + 32);
      float d = 0.f;
#pragma unroll
      for (int i = 0; i < 4; i++) {
        d = dot2(qv[i], a[i], d);
        d = dot2(qv[4 + i], bk[i], d);
      }
      d = dot2(qv[8], k8, d);
      lg[w] = (s0 + tl - 2 * w >= 0) ? d * QSCALE : -3.0e38f;
      m = fmaxf(m, lg[w]);
    }
    float sum = 0.f;
#pragma unroll
    for (int w = 0; w < 8; w++) { lg[w] = __builtin_amdgcn_exp2f(lg[w] - m); sum += lg[w]; }
    const float inv = 1.0f / sum;
    f32x2 ov[9];
#pragma unroll
    for (int i = 0; i < 9; i++) ov[i] = f32x2{0.f, 0.f};
#pragma unroll
    for (int w = 0; w < 8; w++) {
      const unsigned va = lds0 + A_V + (ts - 2 * w) * A_VS + h * 80;
      const float pw = lg[w] * inv;
#pragma unroll
      for (int i = 0; i < 4; i++) {
        f32x4 v = lds_ld<f32x4>(va + i * 16);
        ov[2 * i] += pw * f32x2{v[0], v[1]};
        ov[2 * i + 1] += pw * f32x2{v[2], v[3]};
      }
      ov[8] += pw * lds_ld<f32x2>(va + 64);
    }
    uint16_t* dst = o + (s0 + tl) * DM + hp * 36 + h * 18;
    u32x4 w0 = {pk_bf16(ov[0][0], ov[0][1]), pk_bf16(ov[1][0], ov[1][1]), pk_bf16(ov[2][0], ov[2][1]), pk_bf16(ov[3][0], ov[3][1])};
    u32x4 w1 = {pk_bf16(ov[4][0], ov[4][1]), pk_bf16(ov[5][0], ov[5][1]), pk_bf16(ov[6][0], ov[6][1]), pk_bf16(ov[7][0], ov[7][1])};
    *(u32x4*)(dst) = w0;
    *(u32x4*)(dst + 8) = w1;
    *(unsigned*)(dst + 16) = pk_bf16(ov[8][0], ov[8][1]);
  }
}

extern "C" void kernel_launch(void* const* d_in, const int* in_sizes, int n_in, void* d_out, int out_size,
                              void* d_ws, size_t ws_size, hipStream_t stream) {
  const uint16_t* x0 = (const uint16_t*)d_in[0];
  const uint16_t* wqkv = (const uint16_t*)d_in[1];
  const uint16_t* woA = (const uint16_t*)d_in[2];
  const uint16_t* wq = (const uint16_t*)d_in[4];
  const uint16_t* wk = (const uint16_t*)d_in[5];
  const uint16_t* wv = (const uint16_t*)d_in[6];
  const uint16_t* woB = (const uint16_t*)d_in[7];
  char* ws = (char*)d_ws;
  uint16_t* oA = (uint16_t*)(ws + WS_OA);
  uint16_t* x1 = (uint16_t*)(ws + WS_X1);
  uint16_t* x2 = (uint16_t*)(ws + WS_X2);
  uint16_t* x3 = (uint16_t*)(ws + WS_X3);
  uint16_t* q = (uint16_t*)(ws + WS_Q);
  uint16_t* oB = (uint16_t*)(ws + WS_OB);
  char* kv = ws + WS_KV;
  float* cs2 = (float*)(ws + WS_CS);
  (void)hipFuncSetAttribute((const void*)k_wo, hipFuncAttributeMaxDynamicSharedMemorySize, WO_LDS);
  (void)hipFuncSetAttribute((const void*)k_proj, hipFuncAttributeMaxDynamicSharedMemorySize, PJ_LDS);
  (void)hipFuncSetAttribute((const void*)k_attn, hipFuncAttributeMaxDynamicSharedMemorySize, AT_LDS);
  (void)hipFuncSetAttribute((const void*)k_local, hipFuncAttributeMaxDynamicSharedMemorySize, A_LDS);
  const uint16_t* xin = x0;
  for (int layer = 0; layer < 2; layer++) {
    uint16_t* xa = layer == 0 ? x1 : x3;
    uint16_t* xb = layer == 0 ? x2 : (uint16_t*)d_out;
#define NKERN 10
    if (layer * 5 + 0 < NKERN) k_local<<<256, 512, A_LDS, stream>>>(xin, wqkv, oA, wq, wk, wv, cs2, layer == 0 ? 1 : 0);
    if (layer * 5 + 1 < NKERN) k_wo<<<256, 448, WO_LDS, stream>>>(oA, woA, xin, xa);
    if (layer * 5 + 2 < NKERN) k_proj<<<256, 512, PJ_LDS, stream>>>(xa, wq, wk, wv, cs2, q, kv);
    if (layer * 5 + 3 < NKERN) k_attn<<<256, 1024, AT_LDS, stream>>>(q, kv, oB);
    if (layer * 5 + 4 < NKERN) k_wo<<<256, 448, WO_LDS, stream>>>(oB, woB, xa, xb);
    xin = xb;
  }
}
```

```cpp
#include <hip/hip_runtime.h>
#include <hip/hip_bf16.h>
#include <stdint.h>

#define LAS __attribute__((address_space(3)))
#define GAS __attribute__((address_space(1)))
#define DI __device__ __forceinline__
typedef GAS unsigned gu32;

typedef __attribute__((ext_vector_type(8))) __bf16 bf16x8;
typedef __attribute__((ext_vector_type(2))) __bf16 bf16x2;
typedef __attribute__((ext_vector_type(4))) short s16x4;
typedef __attribute__((ext_vector_type(8))) short s16x8;
typedef __attribute__((ext_vector_type(2))) float f32x2;
typedef __attribute__((ext_vector_type(4))) float f32x4;
typedef __attribute__((ext_vector_type(8))) float f32x8;
typedef __attribute__((ext_vector_type(16))) float f32x16;
typedef __attribute__((ext_vector_type(2))) unsigned u32x2;
typedef __attribute__((ext_vector_type(4))) unsigned u32x4;
typedef __amdgpu_buffer_rsrc_t rsrc_t;

constexpr int SEQ = 2048;
constexpr int DM = 576;
constexpr float LN_EPS = 1e-5f;
constexpr float QSCALE = 0.23570226039551584f * 1.4426950408889634f;
constexpr int AUX_SC1 = 16;
constexpr unsigned OOB = 0x80000000u;

constexpr unsigned T16_TILE = 18432;
constexpr unsigned ACT = SEQ * DM * 2;

constexpr int KV_TILE = 2368, KV_KX = 1024, KV_V = 1152, KV_VG = 608, KV_VH = 304;
constexpr int KV_BYTES = 64 * KV_TILE;

constexpr unsigned WS_OA = 0;
constexpr unsigned WS_OB = 2 * ACT;
constexpr unsigned WS_X1 = 4 * ACT, WS_X2 = 5 * ACT, WS_X3 = 6 * ACT;
constexpr unsigned WS_KV = 7 * ACT;
constexpr unsigned WS_FLAGS = WS_KV + 2 * 262144;
constexpr int FS = 16;
constexpr unsigned WS_EPOCH = WS_FLAGS + 12 * 256 * FS * 4;
constexpr unsigned TA_BYTES = 129024, TW_BYTES = 92160;
constexpr unsigned WS_TA = WS_EPOCH + 4096;
constexpr unsigned WS_TW = WS_TA + 16 * TA_BYTES;
constexpr unsigned WS_X0 = WS_TW + 24 * TW_BYTES;
constexpr unsigned WS_ST = WS_X0 + ACT, ST_BYTES = SEQ * 64;
constexpr unsigned SPIN_CAP = 1u << 16;
constexpr int POLL_SLEEP = 2;

DI int get_lane() { int l; asm volatile("v_mbcnt_lo_u32_b32 %0, -1, 0\n\tv_mbcnt_hi_u32_b32 %0, -1, %0" : "=v"(l)); return l; }
extern __shared__ __attribute__((aligned(16))) char dyn_smem[];
DI unsigned lds_addr(const void* p) { return (unsigned)(uintptr_t)(LAS const char*)p; }
DI const void* uni_ptr(const void* p) {
  const uint64_t a = (uint64_t)p;
  const unsigned lo = __builtin_amdgcn_readfirstlane((unsigned)a), hi = __builtin_amdgcn_readfirstlane((unsigned)(a >> 32));
  return (const void*)(((uint64_t)hi << 32) | lo);
}
DI void glds16_sc1(const void* base, unsigned voff, unsigned lds_dst) {
  unsigned keep;
  asm volatile("s_mov_b32 %0, m0\n\ts_mov_b32 m0, %3\n\ts_nop 3\n\tglobal_load_lds_dwordx4 %1, %2 sc1\n\ts_mov_b32 m0, %0"
               : "=&s"(keep) : "v"(voff), "s"(uni_ptr(base)), "s"(__builtin_amdgcn_readfirstlane(lds_dst)) : "memory");
}
template <int N> DI void wait_vm() { asm volatile("s_waitcnt vmcnt(%0)" ::"i"(N) : "memory"); }
#define SB() __builtin_amdgcn_sched_barrier(0)
DI void bar() { asm volatile("s_barrier" ::: "memory"); }
DI void bar_lgkm() { asm volatile("s_waitcnt lgkmcnt(0)\n\ts_barrier" ::: "memory"); }

DI rsrc_t mk_rsrc(const void* p, unsigned bytes) {
  const uint64_t a = (uint64_t)p;
  const unsigned lo = __builtin_amdgcn_readfirstlane((unsigned)a), hi = __builtin_amdgcn_readfirstlane((unsigned)(a >> 32));
  return __builtin_amdgcn_make_buffer_rsrc((void*)(((uint64_t)hi << 32) | lo), 0, bytes, 0x00020000);
}
DI u32x4 bld128(rsrc_t r, unsigned voff, unsigned soff) {
  return __builtin_amdgcn_raw_buffer_load_b128(r, voff, __builtin_amdgcn_readfirstlane(soff), AUX_SC1);
}
DI u32x2 bld64(rsrc_t r, unsigned voff) { return __builtin_amdgcn_raw_buffer_load_b64(r, voff, 0, AUX_SC1); }
DI void bst128(rsrc_t r, unsigned voff, u32x4 v) { __builtin_amdgcn_raw_buffer_store_b128(v, r, voff, 0, AUX_SC1); }
DI void bst64(rsrc_t r, unsigned voff, u32x2 v) { __builtin_amdgcn_raw_buffer_store_b64(v, r, voff, 0, AUX_SC1); }
DI void bst32(rsrc_t r, unsigned voff, unsigned v) { __builtin_amdgcn_raw_buffer_store_b32(v, r, voff, 0, AUX_SC1); }

DI s16x4 tr_read(unsigned a) { return __builtin_amdgcn_ds_read_tr16_b64_v4i16((LAS s16x4*)(uintptr_t)a); }
DI bf16x8 tr_frag(unsigned a0, unsigned a1) {
  s16x4 lo = tr_read(a0), hi = tr_read(a1);
  s16x8 v = {lo[0], lo[1], lo[2], lo[3], hi[0], hi[1], hi[2], hi[3]};
  return __builtin_bit_cast(bf16x8, v);
}
template <class T> DI T lds_ld(unsigned a) { return *(LAS const T*)(uintptr_t)a; }
template <class T> DI void lds_st(unsigned a, T v) { *(LAS T*)(uintptr_t)a = v; }

DI unsigned pk_bf16(float a, float b) {
  f32x2 v = {a, b};
  return __builtin_bit_cast(unsigned, __builtin_convertvector(v, bf16x2));
}
DI float bf_lo(unsigned u) { return __uint_as_float(u << 16); }
DI float bf_hi(unsigned u) { return __uint_as_float(u & 0xffff0000u); }
DI unsigned vconst(unsigned v) { unsigned r; asm volatile("v_mov_b32 %0, %1" : "=v"(r) : "s"(v)); return r; }
DI u32x4 ones4() { const unsigned o = vconst(0x3F803F80u); return u32x4{o, o, o, o}; }
DI bf16x8 as_frag(u32x4 v) { return __builtin_bit_cast(bf16x8, v); }
DI f32x4 mfma16(bf16x8 a, bf16x8 b, f32x4 c) { return __builtin_amdgcn_mfma_f32_16x16x32_bf16(a, b, c, 0, 0, 0); }
DI f32x16 mfma32(bf16x8 a, bf16x8 b, f32x16 c) { return __builtin_amdgcn_mfma_f32_32x32x16_bf16(a, b, c, 0, 0, 0); }
DI f32x16 mfma32x8(u32x2 a, u32x2 b, f32x16 c) {
  return __builtin_amdgcn_mfma_f32_32x32x8bf16_1k(__builtin_bit_cast(s16x4, a), __builtin_bit_cast(s16x4, b), c, 0, 0, 0);
}
DI float lane_pull(float v, int src) { return __int_as_float(__builtin_amdgcn_ds_bpermute(src * 4, __float_as_int(v))); }
DI float sum_x16(float v) {
  const auto s = __builtin_amdgcn_permlane16_swap(__float_as_uint(v), __float_as_uint(v), false, false);
  return __uint_as_float(s[0]) + __uint_as_float(s[1]);
}
DI float sum_x32(float v) {
  const auto s = __builtin_amdgcn_permlane32_swap(__float_as_uint(v), __float_as_uint(v), false, false);
  return __uint_as_float(s[0]) + __uint_as_float(s[1]);
}
DI float max_x16(float v) {
  const auto s = __builtin_amdgcn_permlane16_swap(__float_as_uint(v), __float_as_uint(v), false, false);
  return fmaxf(__uint_as_float(s[0]), __uint_as_float(s[1]));
}
DI float max_x32(float v) {
  const auto s = __builtin_amdgcn_permlane32_swap(__float_as_uint(v), __float_as_uint(v), false, false);
  return fmaxf(__uint_as_float(s[0]), __uint_as_float(s[1]));
}
DI float sum_8(float v) {
  v += __int_as_float(__builtin_amdgcn_update_dpp(0, __float_as_int(v), 0xB1, 0xf, 0xf, true));
  v += __int_as_float(__builtin_amdgcn_update_dpp(0, __float_as_int(v), 0x4E, 0xf, 0xf, true));
  return v + __int_as_float(__builtin_amdgcn_update_dpp(0, __float_as_int(v), 0x141, 0xf, 0xf, true));
}
DI void acc_stats(unsigned u, float& s1, float& s2) {
  const float a = bf_lo(u), b = bf_hi(u);
  s1 += a + b;
  s2 = fmaf(a, a, fmaf(b, b, s2));
}
DI u32x4 stats_load(rsrc_t r, unsigned row_off, int g) { return bld128(r, row_off + g * 16, 0); }
DI f32x2 stats_finish(u32x4 v) {
  const unsigned a = v[0], b = v[1], c = v[2], d = v[3];
  const float s1 = sum_x32(sum_x16(__uint_as_float(a) + __uint_as_float(c)));
  const float s2 = sum_x32(sum_x16(__uint_as_float(b) + __uint_as_float(d)));
  const float mean = s1 * (1.0f / DM);
  return f32x2{mean, __builtin_amdgcn_rsqf(fmaxf(s2 * (1.0f / DM) - mean * mean, 0.f) + LN_EPS)};
}
DI float gram_diag(f32x4 G, int lane) {
  const int c = lane & 15;
  const float v = (c & 2) ? ((c & 1) ? G[3] : G[2]) : ((c & 1) ? G[1] : G[0]);
  return lane_pull(v, ((c >> 2) << 4) + c);
}
DI unsigned t16_off(int n0, int c) { return (n0 >> 5) * 1024 + ((n0 >> 3) & 3) * 256 + c * 16 + (n0 & 7) * 2; }

DI void poll_one(const unsigned* word, unsigned tag) {
  gu32* f = (gu32*)word;
#pragma unroll 1
  for (unsigned spin = 0; spin < SPIN_CAP; spin++) {
    const unsigned v = __hip_atomic_load(f, __ATOMIC_RELAXED, __HIP_MEMORY_SCOPE_AGENT);
    if (__ballot(v != tag) == 0ull) break;
    __builtin_amdgcn_s_sleep(POLL_SLEEP);
  }
  asm volatile("" ::: "memory");
}
DI void poll_flags(const unsigned* fl, int n, unsigned tag, int lane) { poll_one(fl + (lane < n ? lane : 0) * FS, tag); }
DI void wait_flags(const unsigned* fl, int n, unsigned tag, int lane, int wave, unsigned rdy, unsigned seq) {
  LAS unsigned* r = (LAS unsigned*)(uintptr_t)rdy;
  if (wave == 0) {
    poll_flags(fl, n, tag, lane);
    __hip_atomic_store(r, seq, __ATOMIC_RELAXED, __HIP_MEMORY_SCOPE_WORKGROUP);
  } else {
#pragma unroll 1
    for (unsigned spin = 0; spin < (SPIN_CAP << 4); spin++) {
      if (__hip_atomic_load(r, __ATOMIC_RELAXED, __HIP_MEMORY_SCOPE_WORKGROUP) >= seq) break;
      __builtin_amdgcn_s_sleep(1);
    }
  }
  asm volatile("" ::: "memory");
}
#define ATTN_LOOP_ASM \
  "ds_read_b128 v[168:171], %[ka]\n\t" \
  "ds_read_b32 v172, %[kxa]\n\t" \
  "v_mov_b32 v173, 0\n\t" \
  "v_mov_b32 v164, 0\n\t" \
  "v_mov_b32 v165, 0\n\t" \
  "v_mov_b32 v166, 0\n\t" \
  "v_mov_b32 v167, 0\n\t" \
  "s_waitcnt lgkmcnt(0)\n\t" \
  "v_mfma_f32_32x32x16_bf16 v[128:143], v[168:171], %[qf], 0\n\t" \
  "v_mfma_f32_32x32x8_bf16 v[128:143], v[172:173], %[qx], v[128:143]\n\t" \
  "ds_read_b128 v[168:171], %[ka] offset:2368\n\t" \
  "ds_read_b32 v172, %[kxa] offset:2368\n\t" \
  "ds_read_b128 v[178:181], %[va] offset:608\n\t" \
  "ds_read_b128 v[174:177], %[va]\n\t" \
  "s_mov_b32 %[cnt], 8\n\t" \
  "s_nop 15\n\t" \
  "s_nop 7\n\t" \
  "1:\n\t" \
  "s_waitcnt lgkmcnt(3)\n\t" \
  "v_mfma_f32_32x32x16_bf16 v[144:159], v[168:171], %[qf], 0\n\t" \
  "ds_read_b128 v[168:171], %[ka] offset:4736\n\t" \
  "v_exp_f32 v128, v128\n\t" \
  "v_exp_f32 v129, v129\n\t" \
  "v_exp_f32 v130, v130\n\t" \
  "v_exp_f32 v131, v131\n\t" \
  "v_cvt_pk_bf16_f32 v160, v128, v129\n\t" \
  "v_cvt_pk_bf16_f32 v161, v130, v131\n\t" \
  "s_waitcnt lgkmcnt(3)\n\t" \
  "v_mfma_f32_32x32x8_bf16 v[144:159], v[172:173], %[qx], v[144:159]\n\t" \
  "ds_read_b32 v172, %[kxa] offset:4736\n\t" \
  "v_exp_f32 v132, v132\n\t" \
  "v_exp_f32 v133, v133\n\t" \
  "v_exp_f32 v134, v134\n\t" \
  "v_exp_f32 v135, v135\n\t" \
  "v_cvt_pk_bf16_f32 v162, v132, v133\n\t" \
  "v_cvt_pk_bf16_f32 v163, v134, v135\n\t" \
  "s_waitcnt lgkmcnt(3)\n\t" \
  "v_mfma_f32_32x32x16_bf16 %[o], v[178:181], v[164:167], %[o]\n\t" \
  "ds_read_b128 v[178:181], %[va] offset:608\n\t" \
  "v_exp_f32 v136, v136\n\t" \
  "v_exp_f32 v137, v137\n\t" \
  "v_exp_f32 v138, v138\n\t" \
  "v_exp_f32 v139, v139\n\t" \
  "v_cvt_pk_bf16_f32 v164, v136, v137\n\t" \
  "v_cvt_pk_bf16_f32 v165, v138, v139\n\t" \
  "s_waitcnt lgkmcnt(3)\n\t" \
  "v_mfma_f32_32x32x16_bf16 %[o], v[174:177], v[160:163], %[o]\n\t" \
  "ds_read_b128 v[174:177], %[va] offset:2368\n\t" \
  "v_exp_f32 v140, v140\n\t" \
  "v_exp_f32 v141, v141\n\t" \
  "v_exp_f32 v142, v142\n\t" \
  "v_exp_f32 v143, v143\n\t" \
  "v_cvt_pk_bf16_f32 v166, v140, v141\n\t" \
  "v_cvt_pk_bf16_f32 v167, v142, v143\n\t" \
  "s_waitcnt lgkmcnt(3)\n\t" \
  "v_mfma_f32_32x32x16_bf16 v[128:143], v[168:171], %[qf], 0\n\t" \
  "ds_read_b128 v[168:171], %[ka] offset:7104\n\t" \
  "v_exp_f32 v144, v144\n\t" \
  "v_exp_f32 v145, v145\n\t" \
  "v_exp_f32 v146, v146\n\t" \
  "v_exp_f32 v147, v147\n\t" \
  "v_cvt_pk_bf16_f32 v160, v144, v145\n\t" \
  "v_cvt_pk_bf16_f32 v161, v146, v147\n\t" \
  "s_waitcnt lgkmcnt(3)\n\t" \
  "v_mfma_f32_32x32x8_bf16 v[128:143], v[172:173], %[qx], v[128:143]\n\t" \
  "ds_read_b32 v172, %[kxa] offset:7104\n\t" \
  "v_exp_f32 v148, v148\n\t" \
  "v_exp_f32 v149, v149\n\t" \
  "v_exp_f32 v150, v150\n\t" \
  "v_exp_f32 v151, v151\n\t" \
  "v_cvt_pk_bf16_f32 v162, v148, v149\n\t" \
  "v_cvt_pk_bf16_f32 v163, v150, v151\n\t" \
  "s_waitcnt lgkmcnt(3)\n\t" \
  "v_mfma_f32_32x32x16_bf16 %[o], v[178:181], v[164:167], %[o]\n\t" \
  "ds_read_b128 v[178:181], %[va] offset:2976\n\t" \
  "v_exp_f32 v152, v152\n\t" \
  "v_exp_f32 v153, v153\n\t" \
  "v_exp_f32 v154, v154\n\t" \
  "v_exp_f32 v155, v155\n\t" \
  "v_cvt_pk_bf16_f32 v164, v152, v153\n\t" \
  "v_cvt_pk_bf16_f32 v165, v154, v155\n\t" \
  "s_waitcnt lgkmcnt(3)\n\t" \
  "v_mfma_f32_32x32x16_bf16 %[o], v[174:177], v[160:163], %[o]\n\t" \
  "ds_read_b128 v[174:177], %[va] offset:4736\n\t" \
  "v_exp_f32 v156, v156\n\t" \
  "v_exp_f32 v157, v157\n\t" \
  "v_exp_f32 v158, v158\n\t" \
  "v_exp_f32 v159, v159\n\t" \
  "v_cvt_pk_bf16_f32 v166, v156, v157\n\t" \
  "v_cvt_pk_bf16_f32 v167, v158, v159\n\t" \
  "s_waitcnt lgkmcnt(3)\n\t" \
  "v_mfma_f32_32x32x16_bf16 v[144:159], v[168:171], %[qf], 0\n\t" \
  "ds_read_b128 v[168:171], %[ka] offset:9472\n\t" \
  "v_exp_f32 v128, v128\n\t" \
  "v_exp_f32 v129, v129\n\t" \
  "v_exp_f32 v130, v130\n\t" \
  "v_exp_f32 v131, v131\n\t" \
  "v_cvt_pk_bf16_f32 v160, v128, v129\n\t" \
  "v_cvt_pk_bf16_f32 v161, v130, v131\n\t" \
  "s_waitcnt lgkmcnt(3)\n\t" \
  "v_mfma_f32_32x32x8_bf16 v[144:159], v[172:173], %[qx], v[144:159]\n\t" \
  "ds_read_b32 v172, %[kxa] offset:9472\n\t" \
  "v_exp_f32 v132, v132\n\t" \
  "v_exp_f32 v133, v133\n\t" \
  "v_exp_f32 v134, v134\n\t" \
  "v_exp_f32 v135, v135\n\t" \
  "v_cvt_pk_bf16_f32 v162, v132, v133\n\t" \
  "v_cvt_pk_bf16_f32 v163, v134, v135\n\t" \
  "s_waitcnt lgkmcnt(3)\n\t" \
  "v_mfma_f32_32x32x16_bf16 %[o], v[178:181], v[164:167], %[o]\n\t" \
  "ds_read_b128 v[178:181], %[va] offset:5344\n\t" \
  "v_exp_f32 v136, v136\n\t" \
  "v_exp_f32 v137, v137\n\t" \
  "v_exp_f32 v138, v138\n\t" \
  "v_exp_f32 v139, v139\n\t" \
  "v_cvt_pk_bf16_f32 v164, v136, v137\n\t" \
  "v_cvt_pk_bf16_f32 v165, v138, v139\n\t" \
  "s_waitcnt lgkmcnt(3)\n\t" \
  "v_mfma_f32_32x32x16_bf16 %[o], v[174:177], v[160:163], %[o]\n\t" \
  "ds_read_b128 v[174:177], %[va] offset:7104\n\t" \
  "v_exp_f32 v140, v140\n\t" \
  "v_exp_f32 v141, v141\n\t" \
  "v_exp_f32 v142, v142\n\t" \
  "v_exp_f32 v143, v143\n\t" \
  "v_cvt_pk_bf16_f32 v166, v140, v141\n\t" \
  "v_cvt_pk_bf16_f32 v167, v142, v143\n\t" \
  "s_waitcnt lgkmcnt(3)\n\t" \
  "v_mfma_f32_32x32x16_bf16 v[128:143], v[168:171], %[qf], 0\n\t" \
  "ds_read_b128 v[168:171], %[ka] offset:11840\n\t" \
  "v_exp_f32 v144, v144\n\t" \
  "v_exp_f32 v145, v145\n\t" \
  "v_exp_f32 v146, v146\n\t" \
  "v_exp_f32 v147, v147\n\t" \
  "v_cvt_pk_bf16_f32 v160, v144, v145\n\t" \
  "v_cvt_pk_bf16_f32 v161, v146, v147\n\t" \
  "s_waitcnt lgkmcnt(3)\n\t" \
  "v_mfma_f32_32x32x8_bf16 v[128:143], v[172:173], %[qx], v[128:143]\n\t" \
  "ds_read_b32 v172, %[kxa] offset:11840\n\t" \
  "v_exp_f32 v148, v148\n\t" \
  "v_exp_f32 v149, v149\n\t" \
  "v_exp_f32 v150, v150\n\t" \
  "v_exp_f32 v151, v151\n\t" \
  "v_cvt_pk_bf16_f32 v162, v148, v149\n\t" \
  "v_cvt_pk_bf16_f32 v163, v150, v151\n\t" \
  "s_waitcnt lgkmcnt(3)\n\t" \
  "v_mfma_f32_32x32x16_bf16 %[o], v[178:181], v[164:167], %[o]\n\t" \
  "ds_read_b128 v[178:181], %[va] offset:7712\n\t" \
  "v_exp_f32 v152, v152\n\t" \
  "v_exp_f32 v153, v153\n\t" \
  "v_exp_f32 v154, v154\n\t" \
  "v_exp_f32 v155, v155\n\t" \
  "v_cvt_pk_bf16_f32 v164, v152, v153\n\t" \
  "v_cvt_pk_bf16_f32 v165, v154, v155\n\t" \
  "s_waitcnt lgkmcnt(3)\n\t" \
  "v_mfma_f32_32x32x16_bf16 %[o], v[174:177], v[160:163], %[o]\n\t" \
  "ds_read_b128 v[174:177], %[va] offset:9472\n\t" \
  "v_exp_f32 v156, v156\n\t" \
  "v_exp_f32 v157, v157\n\t" \
  "v_exp_f32 v158, v158\n\t" \
  "v_exp_f32 v159, v159\n\t" \
  "v_cvt_pk_bf16_f32 v166, v156, v157\n\t" \
  "v_cvt_pk_bf16_f32 v167, v158, v159\n\t" \
  "s_waitcnt lgkmcnt(3)\n\t" \
  "v_mfma_f32_32x32x16_bf16 v[144:159], v[168:171], %[qf], 0\n\t" \
  "ds_read_b128 v[168:171], %[ka] offset:14208\n\t" \
  "v_exp_f32 v128, v128\n\t" \
  "v_exp_f32 v129, v129\n\t" \
  "v_exp_f32 v130, v130\n\t" \
  "v_exp_f32 v131, v131\n\t" \
  "v_cvt_pk_bf16_f32 v160, v128, v129\n\t" \
  "v_cvt_pk_bf16_f32 v161, v130, v131\n\t" \
  "s_waitcnt lgkmcnt(3)\n\t" \
  "v_mfma_f32_32x32x8_bf16 v[144:159], v[172:173], %[qx], v[144:159]\n\t" \
  "ds_read_b32 v172, %[kxa] offset:14208\n\t" \
  "v_exp_f32 v132, v132\n\t" \
  "v_exp_f32 v133, v133\n\t" \
  "v_exp_f32 v134, v134\n\t" \
  "v_exp_f32 v135, v135\n\t" \
  "v_cvt_pk_bf16_f32 v162, v132, v133\n\t" \
  "v_cvt_pk_bf16_f32 v163, v134, v135\n\t" \
  "s_waitcnt lgkmcnt(3)\n\t" \
  "v_mfma_f32_32x32x16_bf16 %[o], v[178:181], v[164:167], %[o]\n\t" \
  "ds_read_b128 v[178:181], %[va] offset:10080\n\t" \
  "v_exp_f32 v136, v136\n\t" \
  "v_exp_f32 v137, v137\n\t" \
  "v_exp_f32 v138, v138\n\t" \
  "v_exp_f32 v139, v139\n\t" \
  "v_cvt_pk_bf16_f32 v164, v136, v137\n\t" \
  "v_cvt_pk_bf16_f32 v165, v138, v139\n\t" \
  "s_waitcnt lgkmcnt(3)\n\t" \
  "v_mfma_f32_32x32x16_bf16 %[o], v[174:177], v[160:163], %[o]\n\t" \
  "ds_read_b128 v[174:177], %[va] offset:11840\n\t" \
  "v_exp_f32 v140, v140\n\t" \
  "v_exp_f32 v141, v141\n\t" \
  "v_exp_f32 v142, v142\n\t" \
  "v_exp_f32 v143, v143\n\t" \
  "v_cvt_pk_bf16_f32 v166, v140, v141\n\t" \
  "v_cvt_pk_bf16_f32 v167, v142, v143\n\t" \
  "s_waitcnt lgkmcnt(3)\n\t" \
  "v_mfma_f32_32x32x16_bf16 v[128:143], v[168:171], %[qf], 0\n\t" \
  "ds_read_b128 v[168:171], %[ka] offset:16576\n\t" \
  "v_exp_f32 v144, v144\n\t" \
  "v_exp_f32 v145, v145\n\t" \
  "v_exp_f32 v146, v146\n\t" \
  "v_exp_f32 v147, v147\n\t" \
  "v_cvt_pk_bf16_f32 v160, v144, v145\n\t" \
  "v_cvt_pk_bf16_f32 v161, v146, v147\n\t" \
  "s_waitcnt lgkmcnt(3)\n\t" \
  "v_mfma_f32_32x32x8_bf16 v[128:143], v[172:173], %[qx], v[128:143]\n\t" \
  "ds_read_b32 v172, %[kxa] offset:16576\n\t" \
  "v_exp_f32 v148, v148\n\t" \
  "v_exp_f32 v149, v149\n\t" \
  "v_exp_f32 v150, v150\n\t" \
  "v_exp_f32 v151, v151\n\t" \
  "v_cvt_pk_bf16_f32 v162, v148, v149\n\t" \
  "v_cvt_pk_bf16_f32 v163, v150, v151\n\t" \
  "s_waitcnt lgkmcnt(3)\n\t" \
  "v_mfma_f32_32x32x16_bf16 %[o], v[178:181], v[164:167], %[o]\n\t" \
  "ds_read_b128 v[178:181], %[va] offset:12448\n\t" \
  "v_exp_f32 v152, v152\n\t" \
  "v_exp_f32 v153, v153\n\t" \
  "v_exp_f32 v154, v154\n\t" \
  "v_exp_f32 v155, v155\n\t" \
  "v_cvt_pk_bf16_f32 v164, v152, v153\n\t" \
  "v_cvt_pk_bf16_f32 v165, v154, v155\n\t" \
  "s_waitcnt lgkmcnt(3)\n\t" \
  "v_mfma_f32_32x32x16_bf16 %[o], v[174:177], v[160:163], %[o]\n\t" \
  "ds_read_b128 v[174:177], %[va] offset:14208\n\t" \
  "v_exp_f32 v156, v156\n\t" \
  "v_exp_f32 v157, v157\n\t" \
  "v_exp_f32 v158, v158\n\t" \
  "v_exp_f32 v159, v159\n\t" \
  "v_cvt_pk_bf16_f32 v166, v156, v157\n\t" \
  "v_cvt_pk_bf16_f32 v167, v158, v159\n\t" \
  "s_waitcnt lgkmcnt(3)\n\t" \
  "v_mfma_f32_32x32x16_bf16 v[144:159], v[168:171], %[qf], 0\n\t" \
  "ds_read_b128 v[168:171], %[ka] offset:18944\n\t" \
  "v_exp_f32 v128, v128\n\t" \
  "v_exp_f32 v129, v129\n\t" \
  "v_exp_f32 v130, v130\n\t" \
  "v_exp_f32 v131, v131\n\t" \
  "v_cvt_pk_bf16_f32 v160, v128, v129\n\t" \
  "v_cvt_pk_bf16_f32 v161, v130, v131\n\t" \
  "s_waitcnt lgkmcnt(3)\n\t" \
  "v_mfma_f32_32x32x8_bf16 v[144:159], v[172:173], %[qx], v[144:159]\n\t" \
  "ds_read_b32 v172, %[kxa] offset:18944\n\t" \
  "v_exp_f32 v132, v132\n\t" \
  "v_exp_f32 v133, v133\n\t" \
  "v_exp_f32 v134, v134\n\t" \
  "v_exp_f32 v135, v135\n\t" \
  "v_cvt_pk_bf16_f32 v162, v132, v133\n\t" \
  "v_cvt_pk_bf16_f32 v163, v134, v135\n\t" \
  "s_waitcnt lgkmcnt(3)\n\t" \
  "v_mfma_f32_32x32x16_bf16 %[o], v[178:181], v[164:167], %[o]\n\t" \
  "ds_read_b128 v[178:181], %[va] offset:14816\n\t" \
  "v_exp_f32 v136, v136\n\t" \
  "v_exp_f32 v137, v137\n\t" \
  "v_exp_f32 v138, v138\n\t" \
  "v_exp_f32 v139, v139\n\t" \
  "v_cvt_pk_bf16_f32 v164, v136, v137\n\t" \
  "v_cvt_pk_bf16_f32 v165, v138, v139\n\t" \
  "s_waitcnt lgkmcnt(3)\n\t" \
  "v_mfma_f32_32x32x16_bf16 %[o], v[174:177], v[160:163], %[o]\n\t" \
  "ds_read_b128 v[174:177], %[va] offset:16576\n\t" \
  "v_exp_f32 v140, v140\n\t" \
  "v_exp_f32 v141, v141\n\t" \
  "v_exp_f32 v142, v142\n\t" \
  "v_exp_f32 v143, v143\n\t" \
  "v_cvt_pk_bf16_f32 v166, v140, v141\n\t" \
  "v_cvt_pk_bf16_f32 v167, v142, v143\n\t" \
  "ds_read_b32 v183, %[pb]\n\t" \
  "s_waitcnt lgkmcnt(3)\n\t" \
  "v_mfma_f32_32x32x16_bf16 v[128:143], v[168:171], %[qf], 0\n\t" \
  "ds_read_b128 v[168:171], %[ka] offset:21312\n\t" \
  "v_exp_f32 v144, v144\n\t" \
  "v_exp_f32 v145, v145\n\t" \
  "v_exp_f32 v146, v146\n\t" \
  "v_exp_f32 v147, v147\n\t" \
  "v_cvt_pk_bf16_f32 v160, v144, v145\n\t" \
  "v_cvt_pk_bf16_f32 v161, v146, v147\n\t" \
  "s_waitcnt lgkmcnt(3)\n\t" \
  "v_mfma_f32_32x32x8_bf16 v[128:143], v[172:173], %[qx], v[128:143]\n\t" \
  "ds_read_b32 v172, %[kxa] offset:21312\n\t" \
  "v_exp_f32 v148, v148\n\t" \
  "v_exp_f32 v149, v149\n\t" \
  "v_exp_f32 v150, v150\n\t" \
  "v_exp_f32 v151, v151\n\t" \
  "v_cvt_pk_bf16_f32 v162, v148, v149\n\t" \
  "v_cvt_pk_bf16_f32 v163, v150, v151\n\t" \
  "s_waitcnt lgkmcnt(3)\n\t" \
  "v_mfma_f32_32x32x16_bf16 %[o], v[178:181], v[164:167], %[o]\n\t" \
  "ds_read_b128 v[178:181], %[va] offset:17184\n\t" \
  "v_exp_f32 v152, v152\n\t" \
  "v_exp_f32 v153, v153\n\t" \
  "v_exp_f32 v154, v154\n\t" \
  "v_exp_f32 v155, v155\n\t" \
  "v_cvt_pk_bf16_f32 v164, v152, v153\n\t" \
  "v_cvt_pk_bf16_f32 v165, v154, v155\n\t" \
  "s_waitcnt lgkmcnt(3)\n\t" \
  "v_mfma_f32_32x32x16_bf16 %[o], v[174:177], v[160:163], %[o]\n\t" \
  "ds_read_b128 v[174:177], %[va] offset:18944\n\t" \
  "v_exp_f32 v156, v156\n\t" \
  "v_exp_f32 v157, v157\n\t" \
  "v_exp_f32 v158, v158\n\t" \
  "v_exp_f32 v159, v159\n\t" \
  "v_cvt_pk_bf16_f32 v166, v156, v157\n\t" \
  "v_cvt_pk_bf16_f32 v167, v158, v159\n\t" \
  "v_add_u32 %[ka], 18944, %[ka]\n\t" \
  "v_add_u32 %[kxa], 18944, %[kxa]\n\t" \
  "v_add_u32 %[va], 18944, %[va]\n\t" \
  "s_cmp_lg_u32 %[cnt], 7\n\t" \
  "s_cbranch_scc1 2f\n\t" \
  "3:\n\t" \
  "ds_read_b32 v182, %[ca]\n\t" \
  "s_waitcnt lgkmcnt(0)\n\t" \
  "v_readfirstlane_b32 %[sc], v182\n\t" \
  "s_nop 3\n\t" \
  "s_cmp_ge_u32 %[sc], %[tg]\n\t" \
  "s_cbranch_scc0 3b\n\t" \
  "2:\n\t" \
  "s_sub_u32 %[cnt], %[cnt], 1\n\t" \
  "v_mov_b32 v184, %[cnt]\n\t" \
  "v_readfirstlane_b32 %[sc], v183\n\t" \
  "ds_write_b32 %[pa], v184\n\t" \
  "s_nop 2\n\t" \
  "s_cmp_gt_u32 %[cnt], %[sc]\n\t" \
  "s_cbranch_scc1 5f\n\t" \
  "s_setprio 0\n\t" \
  "s_branch 6f\n\t" \
  "5:\n\t" \
  "s_setprio 1\n\t" \
  "6:\n\t" \
  "s_cmp_lg_u32 %[cnt], 0\n\t" \
  "s_cbranch_scc1 1b\n\t" \
  "s_setprio 0\n\t" \
  "s_waitcnt lgkmcnt(0)\n\t" \
  "v_mfma_f32_32x32x16_bf16 %[o], v[178:181], v[164:167], %[o]\n\t" \
  "s_nop 15\n\t" \
  "s_nop 7\n\t"
#define ATTN_LOOP_CLOB "v128", "v129", "v130", "v131", "v132", "v133", "v134", "v135", "v136", "v137", "v138", "v139", "v140", "v141", "v142", "v143", "v144", "v145", "v146", "v147", "v148", "v149", "v150", "v151", "v152", "v153", "v154", "v155", "v156", "v157", "v158", "v159", "v160", "v161", "v162", "v163", "v164", "v165", "v166", "v167", "v168", "v169", "v170", "v171", "v172", "v173", "v174", "v175", "v176", "v177", "v178", "v179", "v180", "v181", "v182", "v183", "v184"
#define A_LOOP_ASM \
  "s_nop 4\n\t" \
  "s_mov_b32 %[so], 0\n\t" \
  "buffer_load_dwordx4 v[160:163], %[xb], %[rs], %[so] offen sc1\n\t" \
  "s_mov_b32 %[so], 18432\n\t" \
  "buffer_load_dwordx4 v[164:167], %[xb], %[rs], %[so] offen sc1\n\t" \
  "s_mov_b32 %[so], 1024\n\t" \
  "buffer_load_dwordx4 v[168:171], %[xb], %[rs], %[so] offen sc1\n\t" \
  "s_mov_b32 %[so], 19456\n\t" \
  "buffer_load_dwordx4 v[172:175], %[xb], %[rs], %[so] offen sc1\n\t" \
  "s_mov_b32 %[so], 2048\n\t" \
  "buffer_load_dwordx4 v[176:179], %[xb], %[rs], %[so] offen sc1\n\t" \
  "s_mov_b32 %[so], 20480\n\t" \
  "buffer_load_dwordx4 v[180:183], %[xb], %[rs], %[so] offen sc1\n\t" \
  "s_mov_b32 %[so], 3072\n\t" \
  "buffer_load_dwordx4 v[184:187], %[xb], %[rs], %[so] offen sc1\n\t" \
  "s_mov_b32 %[so], 21504\n\t" \
  "buffer_load_dwordx4 v[188:191], %[xb], %[rs], %[so] offen sc1\n\t" \
  "s_mov_b32 %[so], 4096\n\t" \
  "buffer_load_dwordx4 v[192:195], %[xb], %[rs], %[so] offen sc1\n\t" \
  "s_mov_b32 %[so], 22528\n\t" \
  "buffer_load_dwordx4 v[196:199], %[xb], %[rs], %[so] offen sc1\n\t" \
  "s_barrier\n\t" \
  "ds_read_b128 v[112:115], %[wa] offset:0\n\t" \
  "ds_read_b128 v[116:119], %[wa] offset:1024\n\t" \
  "ds_read_b128 v[120:123], %[wa] offset:2048\n\t" \
  "ds_read_b128 v[124:127], %[wa] offset:3072\n\t" \
  "ds_read_b128 v[128:131], %[wa] offset:4096\n\t" \
  "ds_read_b128 v[132:135], %[wa] offset:5120\n\t" \
  "ds_read_b128 v[136:139], %[wa] offset:6144\n\t" \
  "ds_read_b128 v[140:143], %[h0] offset:0\n\t" \
  "ds_read_b128 v[144:147], %[h1] offset:0\n\t" \
  "ds_read_b128 v[148:151], %[ha] offset:0\n\t" \
  "s_waitcnt vmcnt(8)\n\t" \
  "s_waitcnt lgkmcnt(8)\n\t" \
  "v_mfma_f32_16x16x32_bf16 v[40:43], v[112:115], v[160:163], 0\n\t" \
  "v_mfma_f32_16x16x32_bf16 v[68:71], v[112:115], v[164:167], 0\n\t" \
  "ds_read_b128 v[152:155], %[ha] offset:1024\n\t" \
  "v_mfma_f32_16x16x32_bf16 v[44:47], v[116:119], v[160:163], 0\n\t" \
  "ds_read_b128 v[112:115], %[wa] offset:7168\n\t" \
  "v_mfma_f32_16x16x32_bf16 v[72:75], v[116:119], v[164:167], 0\n\t" \
  "s_waitcnt lgkmcnt(8)\n\t" \
  "v_mfma_f32_16x16x32_bf16 v[48:51], v[120:123], v[160:163], 0\n\t" \
  "ds_read_b128 v[116:119], %[wa] offset:8192\n\t" \
  "v_mfma_f32_16x16x32_bf16 v[76:79], v[120:123], v[164:167], 0\n\t" \
  "v_mfma_f32_16x16x32_bf16 v[52:55], v[124:127], v[160:163], 0\n\t" \
  "ds_read_b128 v[120:123], %[wa] offset:9216\n\t" \
  "v_mfma_f32_16x16x32_bf16 v[80:83], v[124:127], v[164:167], 0\n\t" \
  "s_waitcnt lgkmcnt(8)\n\t" \
  "v_mfma_f32_16x16x32_bf16 v[56:59], v[128:131], v[160:163], 0\n\t" \
  "ds_read_b128 v[124:127], %[wa] offset:10240\n\t" \
  "v_mfma_f32_16x16x32_bf16 v[84:87], v[128:131], v[164:167], 0\n\t" \
  "v_mfma_f32_16x16x32_bf16 v[60:63], v[132:135], v[160:163], 0\n\t" \
  "ds_read_b128 v[128:131], %[wa] offset:11264\n\t" \
  "v_mfma_f32_16x16x32_bf16 v[88:91], v[132:135], v[164:167], 0\n\t" \
  "s_waitcnt lgkmcnt(9)\n\t" \
  "v_mfma_f32_16x16x32_bf16 v[64:67], v[136:139], v[160:163], 0\n\t" \
  "ds_read_b128 v[132:135], %[wa] offset:12288\n\t" \
  "v_mfma_f32_16x16x32_bf16 v[92:95], v[136:139], v[164:167], 0\n\t" \
  "s_waitcnt lgkmcnt(7)\n\t" \
  "v_mfma_f32_16x16x32_bf16 v[96:99], v[140:143], v[148:151], 0\n\t" \
  "ds_read_b128 v[136:139], %[wa] offset:13312\n\t" \
  "ds_read_b128 v[140:143], %[h0] offset:7168\n\t" \
  "v_mfma_f32_16x16x32_bf16 v[100:103], v[144:147], v[148:151], 0\n\t" \
  "ds_read_b128 v[144:147], %[h1] offset:7168\n\t" \
  "s_mov_b32 %[so], 5120\n\t" \
  "buffer_load_dwordx4 v[200:203], %[xb], %[rs], %[so] offen sc1\n\t" \
  "s_mov_b32 %[so], 23552\n\t" \
  "buffer_load_dwordx4 v[204:207], %[xb], %[rs], %[so] offen sc1\n\t" \
  "s_mov_b32 %[so], 6144\n\t" \
  "buffer_load_dwordx4 v[208:211], %[xb], %[rs], %[so] offen sc1\n\t" \
  "s_mov_b32 %[so], 24576\n\t" \
  "buffer_load_dwordx4 v[212:215], %[xb], %[rs], %[so] offen sc1\n\t" \
  "s_waitcnt vmcnt(10)\n\t" \
  "s_waitcnt lgkmcnt(7)\n\t" \
  "v_mfma_f32_16x16x32_bf16 v[40:43], v[112:115], v[168:171], v[40:43]\n\t" \
  "v_mfma_f32_16x16x32_bf16 v[68:71], v[112:115], v[172:175], v[68:71]\n\t" \
  "ds_read_b128 v[148:151], %[ha] offset:2048\n\t" \
  "v_mfma_f32_16x16x32_bf16 v[44:47], v[116:119], v[168:171], v[44:47]\n\t" \
  "ds_read_b128 v[112:115], %[wa] offset:14336\n\t" \
  "v_mfma_f32_16x16x32_bf16 v[72:75], v[116:119], v[172:175], v[72:75]\n\t" \
  "s_waitcnt lgkmcnt(7)\n\t" \
  "v_mfma_f32_16x16x32_bf16 v[48:51], v[120:123], v[168:171], v[48:51]\n\t" \
  "ds_read_b128 v[116:119], %[wa] offset:15360\n\t" \
  "v_mfma_f32_16x16x32_bf16 v[76:79], v[120:123], v[172:175], v[76:79]\n\t" \
  "v_mfma_f32_16x16x32_bf16 v[52:55], v[124:127], v[168:171], v[52:55]\n\t" \
  "ds_read_b128 v[120:123], %[wa] offset:16384\n\t" \
  "v_mfma_f32_16x16x32_bf16 v[80:83], v[124:127], v[172:175], v[80:83]\n\t" \
  "s_waitcnt lgkmcnt(7)\n\t" \
  "v_mfma_f32_16x16x32_bf16 v[56:59], v[128:131], v[168:171], v[56:59]\n\t" \
  "ds_read_b128 v[124:127], %[wa] offset:17408\n\t" \
  "v_mfma_f32_16x16x32_bf16 v[84:87], v[128:131], v[172:175], v[84:87]\n\t" \
  "v_mfma_f32_16x16x32_bf16 v[60:63], v[132:135], v[168:171], v[60:63]\n\t" \
  "ds_read_b128 v[128:131], %[wa] offset:18432\n\t" \
  "v_mfma_f32_16x16x32_bf16 v[88:91], v[132:135], v[172:175], v[88:91]\n\t" \
  "s_waitcnt lgkmcnt(8)\n\t" \
  "v_mfma_f32_16x16x32_bf16 v[64:67], v[136:139], v[168:171], v[64:67]\n\t" \
  "ds_read_b128 v[132:135], %[wa] offset:19456\n\t" \
  "v_mfma_f32_16x16x32_bf16 v[92:95], v[136:139], v[172:175], v[92:95]\n\t" \
  "s_waitcnt lgkmcnt(7)\n\t" \
  "v_mfma_f32_16x16x32_bf16 v[96:99], v[140:143], v[152:155], v[96:99]\n\t" \
  "ds_read_b128 v[136:139], %[wa] offset:20480\n\t" \
  "ds_read_b128 v[140:143], %[h0] offset:14336\n\t" \
  "v_mfma_f32_16x16x32_bf16 v[100:103], v[144:147], v[152:155], v[100:103]\n\t" \
  "ds_read_b128 v[144:147], %[h1] offset:14336\n\t" \
  "s_mov_b32 %[so], 7168\n\t" \
  "buffer_load_dwordx4 v[216:219], %[xb], %[rs], %[so] offen sc1\n\t" \
  "s_mov_b32 %[so], 25600\n\t" \
  "buffer_load_dwordx4 v[220:223], %[xb], %[rs], %[so] offen sc1\n\t" \
  "s_mov_b32 %[so], 8192\n\t" \
  "buffer_load_dwordx4 v[224:227], %[xb], %[rs], %[so] offen sc1\n\t" \
  "s_mov_b32 %[so], 26624\n\t" \
  "buffer_load_dwordx4 v[228:231], %[xb], %[rs], %[so] offen sc1\n\t" \
  "s_barrier\n\t" \
  "s_waitcnt vmcnt(12)\n\t" \
  "s_waitcnt lgkmcnt(7)\n\t" \
  "v_mfma_f32_16x16x32_bf16 v[40:43], v[112:115], v[176:179], v[40:43]\n\t" \
  "v_mfma_f32_16x16x32_bf16 v[68:71], v[112:115], v[180:183], v[68:71]\n\t" \
  "ds_read_b128 v[152:155], %[ha] offset:3072\n\t" \
  "v_mfma_f32_16x16x32_bf16 v[44:47], v[116:119], v[176:179], v[44:47]\n\t" \
  "ds_read_b128 v[112:115], %[wa] offset:21504\n\t" \
  "v_mfma_f32_16x16x32_bf16 v[72:75], v[116:119], v[180:183], v[72:75]\n\t" \
  "s_waitcnt lgkmcnt(7)\n\t" \
  "v_mfma_f32_16x16x32_bf16 v[48:51], v[120:123], v[176:179], v[48:51]\n\t" \
  "ds_read_b128 v[116:119], %[wa] offset:22528\n\t" \
  "v_mfma_f32_16x16x32_bf16 v[76:79], v[120:123], v[180:183], v[76:79]\n\t" \
  "v_mfma_f32_16x16x32_bf16 v[52:55], v[124:127], v[176:179], v[52:55]\n\t" \
  "ds_read_b128 v[120:123], %[wa] offset:23552\n\t" \
  "v_mfma_f32_16x16x32_bf16 v[80:83], v[124:127], v[180:183], v[80:83]\n\t" \
  "s_waitcnt lgkmcnt(7)\n\t" \
  "v_mfma_f32_16x16x32_bf16 v[56:59], v[128:131], v[176:179], v[56:59]\n\t" \
  "ds_read_b128 v[124:127], %[wa] offset:24576\n\t" \
  "v_mfma_f32_16x16x32_bf16 v[84:87], v[128:131], v[180:183], v[84:87]\n\t" \
  "v_mfma_f32_16x16x32_bf16 v[60:63], v[132:135], v[176:179], v[60:63]\n\t" \
  "ds_read_b128 v[128:131], %[wa] offset:25600\n\t" \
  "v_mfma_f32_16x16x32_bf16 v[88:91], v[132:135], v[180:183], v[88:91]\n\t" \
  "s_waitcnt lgkmcnt(8)\n\t" \
  "v_mfma_f32_16x16x32_bf16 v[64:67], v[136:139], v[176:179], v[64:67]\n\t" \
  "ds_read_b128 v[132:135], %[wa] offset:26624\n\t" \
  "v_mfma_f32_16x16x32_bf16 v[92:95], v[136:139], v[180:183], v[92:95]\n\t" \
  "s_waitcnt lgkmcnt(7)\n\t" \
  "v_mfma_f32_16x16x32_bf16 v[96:99], v[140:143], v[148:151], v[96:99]\n\t" \
  "ds_read_b128 v[136:139], %[wa] offset:27648\n\t" \
  "ds_read_b128 v[140:143], %[h0] offset:21504\n\t" \
  "v_mfma_f32_16x16x32_bf16 v[100:103], v[144:147], v[148:151], v[100:103]\n\t" \
  "ds_read_b128 v[144:147], %[h1] offset:21504\n\t" \
  "s_mov_b32 %[so], 9216\n\t" \
  "buffer_load_dwordx4 v[232:235], %[xb], %[rs], %[so] offen sc1\n\t" \
  "s_mov_b32 %[so], 27648\n\t" \
  "buffer_load_dwordx4 v[236:239], %[xb], %[rs], %[so] offen sc1\n\t" \
  "s_waitcnt vmcnt(12)\n\t" \
  "s_waitcnt lgkmcnt(7)\n\t" \
  "v_mfma_f32_16x16x32_bf16 v[40:43], v[112:115], v[184:187], v[40:43]\n\t" \
  "v_mfma_f32_16x16x32_bf16 v[68:71], v[112:115], v[188:191], v[68:71]\n\t" \
  "ds_read_b128 v[148:151], %[ha] offset:4096\n\t" \
  "v_mfma_f32_16x16x32_bf16 v[44:47], v[116:119], v[184:187], v[44:47]\n\t" \
  "ds_read_b128 v[112:115], %[wa] offset:28672\n\t" \
  "v_mfma_f32_16x16x32_bf16 v[72:75], v[116:119], v[188:191], v[72:75]\n\t" \
  "s_waitcnt lgkmcnt(7)\n\t" \
  "v_mfma_f32_16x16x32_bf16 v[48:51], v[120:123], v[184:187], v[48:51]\n\t" \
  "ds_read_b128 v[116:119], %[wa] offset:29696\n\t" \
  "v_mfma_f32_16x16x32_bf16 v[76:79], v[120:123], v[188:191], v[76:79]\n\t" \
  "v_mfma_f32_16x16x32_bf16 v[52:55], v[124:127], v[184:187], v[52:55]\n\t" \
  "ds_read_b128 v[120:123], %[wa] offset:30720\n\t" \
  "v_mfma_f32_16x16x32_bf16 v[80:83], v[124:127], v[188:191], v[80:83]\n\t" \
  "s_waitcnt lgkmcnt(7)\n\t" \
  "v_mfma_f32_16x16x32_bf16 v[56:59], v[128:131], v[184:187], v[56:59]\n\t" \
  "ds_read_b128 v[124:127], %[wa] offset:31744\n\t" \
  "v_mfma_f32_16x16x32_bf16 v[84:87], v[128:131], v[188:191], v[84:87]\n\t" \
  "v_mfma_f32_16x16x32_bf16 v[60:63], v[132:135], v[184:187], v[60:63]\n\t" \
  "ds_read_b128 v[128:131], %[wa] offset:32768\n\t" \
  "v_mfma_f32_16x16x32_bf16 v[88:91], v[132:135], v[188:191], v[88:91]\n\t" \
  "s_waitcnt lgkmcnt(8)\n\t" \
  "v_mfma_f32_16x16x32_bf16 v[64:67], v[136:139], v[184:187], v[64:67]\n\t" \
  "ds_read_b128 v[132:135], %[wa] offset:33792\n\t" \
  "v_mfma_f32_16x16x32_bf16 v[92:95], v[136:139], v[188:191], v[92:95]\n\t" \
  "s_waitcnt lgkmcnt(7)\n\t" \
  "v_mfma_f32_16x16x32_bf16 v[96:99], v[140:143], v[152:155], v[96:99]\n\t" \
  "ds_read_b128 v[136:139], %[wa] offset:34816\n\t" \
  "ds_read_b128 v[140:143], %[h0] offset:28672\n\t" \
  "v_mfma_f32_16x16x32_bf16 v[100:103], v[144:147], v[152:155], v[100:103]\n\t" \
  "ds_read_b128 v[144:147], %[h1] offset:28672\n\t" \
  "s_mov_b32 %[so], 10240\n\t" \
  "buffer_load_dwordx4 v[240:243], %[xb], %[rs], %[so] offen sc1\n\t" \
  "s_mov_b32 %[so], 28672\n\t" \
  "buffer_load_dwordx4 v[244:247], %[xb], %[rs], %[so] offen sc1\n\t" \
  "s_waitcnt vmcnt(12)\n\t" \
  "s_waitcnt lgkmcnt(7)\n\t" \
  "v_mfma_f32_16x16x32_bf16 v[40:43], v[112:115], v[192:195], v[40:43]\n\t" \
  "v_mfma_f32_16x16x32_bf16 v[68:71], v[112:115], v[196:199], v[68:71]\n\t" \
  "ds_read_b128 v[152:155], %[ha] offset:5120\n\t" \
  "v_mfma_f32_16x16x32_bf16 v[44:47], v[116:119], v[192:195], v[44:47]\n\t" \
  "ds_read_b128 v[112:115], %[wa] offset:35840\n\t" \
  "v_mfma_f32_16x16x32_bf16 v[72:75], v[116:119], v[196:199], v[72:75]\n\t" \
  "s_waitcnt lgkmcnt(7)\n\t" \
  "v_mfma_f32_16x16x32_bf16 v[48:51], v[120:123], v[192:195], v[48:51]\n\t" \
  "ds_read_b128 v[116:119], %[wa] offset:36864\n\t" \
  "v_mfma_f32_16x16x32_bf16 v[76:79], v[120:123], v[196:199], v[76:79]\n\t" \
  "v_mfma_f32_16x16x32_bf16 v[52:55], v[124:127], v[192:195], v[52:55]\n\t" \
  "ds_read_b128 v[120:123], %[wa] offset:37888\n\t" \
  "v_mfma_f32_16x16x32_bf16 v[80:83], v[124:127], v[196:199], v[80:83]\n\t" \
  "s_waitcnt lgkmcnt(7)\n\t" \
  "v_mfma_f32_16x16x32_bf16 v[56:59], v[128:131], v[192:195], v[56:59]\n\t" \
  "ds_read_b128 v[124:127], %[wa] offset:38912\n\t" \
  "v_mfma_f32_16x16x32_bf16 v[84:87], v[128:131], v[196:199], v[84:87]\n\t" \
  "v_mfma_f32_16x16x32_bf16 v[60:63], v[132:135], v[192:195], v[60:63]\n\t" \
  "ds_read_b128 v[128:131], %[wa] offset:39936\n\t" \
  "v_mfma_f32_16x16x32_bf16 v[88:91], v[132:135], v[196:199], v[88:91]\n\t" \
  "s_waitcnt lgkmcnt(8)\n\t" \
  "v_mfma_f32_16x16x32_bf16 v[64:67], v[136:139], v[192:195], v[64:67]\n\t" \
  "ds_read_b128 v[132:135], %[wa] offset:40960\n\t" \
  "v_mfma_f32_16x16x32_bf16 v[92:95], v[136:139], v[196:199], v[92:95]\n\t" \
  "s_waitcnt lgkmcnt(7)\n\t" \
  "v_mfma_f32_16x16x32_bf16 v[96:99], v[140:143], v[148:151], v[96:99]\n\t" \
  "ds_read_b128 v[136:139], %[wa] offset:41984\n\t" \
  "ds_read_b128 v[140:143], %[h0] offset:35840\n\t" \
  "v_mfma_f32_16x16x32_bf16 v[100:103], v[144:147], v[148:151], v[100:103]\n\t" \
  "ds_read_b128 v[144:147], %[h1] offset:35840\n\t" \
  "s_mov_b32 %[so], 11264\n\t" \
  "buffer_load_dwordx4 v[248:251], %[xb], %[rs], %[so] offen sc1\n\t" \
  "s_mov_b32 %[so], 29696\n\t" \
  "buffer_load_dwordx4 v[252:255], %[xb], %[rs], %[so] offen sc1\n\t" \
  "s_barrier\n\t" \
  "s_waitcnt vmcnt(12)\n\t" \
  "s_waitcnt lgkmcnt(7)\n\t" \
  "v_mfma_f32_16x16x32_bf16 v[40:43], v[112:115], v[200:203], v[40:43]\n\t" \
  "v_mfma_f32_16x16x32_bf16 v[68:71], v[112:115], v[204:207], v[68:71]\n\t" \
  "ds_read_b128 v[148:151], %[ha] offset:6144\n\t" \
  "v_mfma_f32_16x16x32_bf16 v[44:47], v[116:119], v[200:203], v[44:47]\n\t" \
  "ds_read_b128 v[112:115], %[wa] offset:43008\n\t" \
  "v_mfma_f32_16x16x32_bf16 v[72:75], v[116:119], v[204:207], v[72:75]\n\t" \
  "s_waitcnt lgkmcnt(7)\n\t" \
  "v_mfma_f32_16x16x32_bf16 v[48:51], v[120:123], v[200:203], v[48:51]\n\t" \
  "ds_read_b128 v[116:119], %[wa] offset:44032\n\t" \
  "v_mfma_f32_16x16x32_bf16 v[76:79], v[120:123], v[204:207], v[76:79]\n\t" \
  "v_mfma_f32_16x16x32_bf16 v[52:55], v[124:127], v[200:203], v[52:55]\n\t" \
  "ds_read_b128 v[120:123], %[wa] offset:45056\n\t" \
  "v_mfma_f32_16x16x32_bf16 v[80:83], v[124:127], v[204:207], v[80:83]\n\t" \
  "s_waitcnt lgkmcnt(7)\n\t" \
  "v_mfma_f32_16x16x32_bf16 v[56:59], v[128:131], v[200:203], v[56:59]\n\t" \
  "ds_read_b128 v[124:127], %[wa] offset:46080\n\t" \
  "v_mfma_f32_16x16x32_bf16 v[84:87], v[128:131], v[204:207], v[84:87]\n\t" \
  "v_mfma_f32_16x16x32_bf16 v[60:63], v[132:135], v[200:203], v[60:63]\n\t" \
  "ds_read_b128 v[128:131], %[wa] offset:47104\n\t" \
  "v_mfma_f32_16x16x32_bf16 v[88:91], v[132:135], v[204:207], v[88:91]\n\t" \
  "s_waitcnt lgkmcnt(8)\n\t" \
  "v_mfma_f32_16x16x32_bf16 v[64:67], v[136:139], v[200:203], v[64:67]\n\t" \
  "ds_read_b128 v[132:135], %[wa] offset:48128\n\t" \
  "v_mfma_f32_16x16x32_bf16 v[92:95], v[136:139], v[204:207], v[92:95]\n\t" \
  "s_waitcnt lgkmcnt(7)\n\t" \
  "v_mfma_f32_16x16x32_bf16 v[96:99], v[140:143], v[152:155], v[96:99]\n\t" \
  "ds_read_b128 v[136:139], %[wa] offset:49152\n\t" \
  "ds_read_b128 v[140:143], %[h0] offset:43008\n\t" \
  "v_mfma_f32_16x16x32_bf16 v[100:103], v[144:147], v[152:155], v[100:103]\n\t" \
  "ds_read_b128 v[144:147], %[h1] offset:43008\n\t" \
  "s_mov_b32 %[so], 12288\n\t" \
  "buffer_load_dwordx4 v[160:163], %[xb], %[rs], %[so] offen sc1\n\t" \
  "s_mov_b32 %[so], 30720\n\t" \
  "buffer_load_dwordx4 v[164:167], %[xb], %[rs], %[so] offen sc1\n\t" \
  "s_waitcnt vmcnt(12)\n\t" \
  "s_waitcnt lgkmcnt(7)\n\t" \
  "v_mfma_f32_16x16x32_bf16 v[40:43], v[112:115], v[208:211], v[40:43]\n\t" \
  "v_mfma_f32_16x16x32_bf16 v[68:71], v[112:115], v[212:215], v[68:71]\n\t" \
  "ds_read_b128 v[152:155], %[ha] offset:7168\n\t" \
  "v_mfma_f32_16x16x32_bf16 v[44:47], v[116:119], v[208:211], v[44:47]\n\t" \
  "ds_read_b128 v[112:115], %[wa] offset:50176\n\t" \
  "v_mfma_f32_16x16x32_bf16 v[72:75], v[116:119], v[212:215], v[72:75]\n\t" \
  "s_waitcnt lgkmcnt(7)\n\t" \
  "v_mfma_f32_16x16x32_bf16 v[48:51], v[120:123], v[208:211], v[48:51]\n\t" \
  "ds_read_b128 v[116:119], %[wa] offset:51200\n\t" \
  "v_mfma_f32_16x16x32_bf16 v[76:79], v[120:123], v[212:215], v[76:79]\n\t" \
  "v_mfma_f32_16x16x32_bf16 v[52:55], v[124:127], v[208:211], v[52:55]\n\t" \
  "ds_read_b128 v[120:123], %[wa] offset:52224\n\t" \
  "v_mfma_f32_16x16x32_bf16 v[80:83], v[124:127], v[212:215], v[80:83]\n\t" \
  "s_waitcnt lgkmcnt(7)\n\t" \
  "v_mfma_f32_16x16x32_bf16 v[56:59], v[128:131], v[208:211], v[56:59]\n\t" \
  "ds_read_b128 v[124:127], %[wa] offset:53248\n\t" \
  "v_mfma_f32_16x16x32_bf16 v[84:87], v[128:131], v[212:215], v[84:87]\n\t" \
  "v_mfma_f32_16x16x32_bf16 v[60:63], v[132:135], v[208:211], v[60:63]\n\t" \
  "ds_read_b128 v[128:131], %[wa] offset:54272\n\t" \
  "v_mfma_f32_16x16x32_bf16 v[88:91], v[132:135], v[212:215], v[88:91]\n\t" \
  "s_waitcnt lgkmcnt(8)\n\t" \
  "v_mfma_f32_16x16x32_bf16 v[64:67], v[136:139], v[208:211], v[64:67]\n\t" \
  "ds_read_b128 v[132:135], %[wa] offset:55296\n\t" \
  "v_mfma_f32_16x16x32_bf16 v[92:95], v[136:139], v[212:215], v[92:95]\n\t" \
  "s_waitcnt lgkmcnt(7)\n\t" \
  "v_mfma_f32_16x16x32_bf16 v[96:99], v[140:143], v[148:151], v[96:99]\n\t" \
  "ds_read_b128 v[136:139], %[wa] offset:56320\n\t" \
  "ds_read_b128 v[140:143], %[h0] offset:50176\n\t" \
  "v_mfma_f32_16x16x32_bf16 v[100:103], v[144:147], v[148:151], v[100:103]\n\t" \
  "ds_read_b128 v[144:147], %[h1] offset:50176\n\t" \
  "s_mov_b32 %[so], 13312\n\t" \
  "buffer_load_dwordx4 v[168:171], %[xb], %[rs], %[so] offen sc1\n\t" \
  "s_mov_b32 %[so], 31744\n\t" \
  "buffer_load_dwordx4 v[172:175], %[xb], %[rs], %[so] offen sc1\n\t" \
  "v_add_u32 %[wa], 57344, %[wa]\n\t" \
  "v_add_u32 %[h0], 57344, %[h0]\n\t" \
  "v_add_u32 %[h1], 57344, %[h1]\n\t" \
  "s_waitcnt vmcnt(12)\n\t" \
  "s_waitcnt lgkmcnt(7)\n\t" \
  "v_mfma_f32_16x16x32_bf16 v[40:43], v[112:115], v[216:219], v[40:43]\n\t" \
  "v_mfma_f32_16x16x32_bf16 v[68:71], v[112:115], v[220:223], v[68:71]\n\t" \
  "ds_read_b128 v[148:151], %[ha] offset:8192\n\t" \
  "v_mfma_f32_16x16x32_bf16 v[44:47], v[116:119], v[216:219], v[44:47]\n\t" \
  "ds_read_b128 v[112:115], %[wa] offset:0\n\t" \
  "v_mfma_f32_16x16x32_bf16 v[72:75], v[116:119], v[220:223], v[72:75]\n\t" \
  "s_waitcnt lgkmcnt(7)\n\t" \
  "v_mfma_f32_16x16x32_bf16 v[48:51], v[120:123], v[216:219], v[48:51]\n\t" \
  "ds_read_b128 v[116:119], %[wa] offset:1024\n\t" \
  "v_mfma_f32_16x16x32_bf16 v[76:79], v[120:123], v[220:223], v[76:79]\n\t" \
  "v_mfma_f32_16x16x32_bf16 v[52:55], v[124:127], v[216:219], v[52:55]\n\t" \
  "ds_read_b128 v[120:123], %[wa] offset:2048\n\t" \
  "v_mfma_f32_16x16x32_bf16 v[80:83], v[124:127], v[220:223], v[80:83]\n\t" \
  "s_waitcnt lgkmcnt(7)\n\t" \
  "v_mfma_f32_16x16x32_bf16 v[56:59], v[128:131], v[216:219], v[56:59]\n\t" \
  "ds_read_b128 v[124:127], %[wa] offset:3072\n\t" \
  "v_mfma_f32_16x16x32_bf16 v[84:87], v[128:131], v[220:223], v[84:87]\n\t" \
  "v_mfma_f32_16x16x32_bf16 v[60:63], v[132:135], v[216:219], v[60:63]\n\t" \
  "ds_read_b128 v[128:131], %[wa] offset:4096\n\t" \
  "v_mfma_f32_16x16x32_bf16 v[88:91], v[132:135], v[220:223], v[88:91]\n\t" \
  "s_waitcnt lgkmcnt(8)\n\t" \
  "v_mfma_f32_16x16x32_bf16 v[64:67], v[136:139], v[216:219], v[64:67]\n\t" \
  "ds_read_b128 v[132:135], %[wa] offset:5120\n\t" \
  "v_mfma_f32_16x16x32_bf16 v[92:95], v[136:139], v[220:223], v[92:95]\n\t" \
  "s_waitcnt lgkmcnt(7)\n\t" \
  "v_mfma_f32_16x16x32_bf16 v[96:99], v[140:143], v[152:155], v[96:99]\n\t" \
  "ds_read_b128 v[136:139], %[wa] offset:6144\n\t" \
  "ds_read_b128 v[140:143], %[h0] offset:0\n\t" \
  "v_mfma_f32_16x16x32_bf16 v[100:103], v[144:147], v[152:155], v[100:103]\n\t" \
  "ds_read_b128 v[144:147], %[h1] offset:0\n\t" \
  "s_mov_b32 %[so], 14336\n\t" \
  "buffer_load_dwordx4 v[176:179], %[xb], %[rs], %[so] offen sc1\n\t" \
  "s_mov_b32 %[so], 32768\n\t" \
  "buffer_load_dwordx4 v[180:183], %[xb], %[rs], %[so] offen sc1\n\t" \
  "s_barrier\n\t" \
  "s_waitcnt vmcnt(12)\n\t" \
  "s_waitcnt lgkmcnt(7)\n\t" \
  "v_mfma_f32_16x16x32_bf16 v[40:43], v[112:115], v[224:227], v[40:43]\n\t" \
  "v_mfma_f32_16x16x32_bf16 v[68:71], v[112:115], v[228:231], v[68:71]\n\t" \
  "ds_read_b128 v[152:155], %[ha] offset:9216\n\t" \
  "v_mfma_f32_16x16x32_bf16 v[44:47], v[116:119], v[224:227], v[44:47]\n\t" \
  "ds_read_b128 v[112:115], %[wa] offset:7168\n\t" \
  "v_mfma_f32_16x16x32_bf16 v[72:75], v[116:119], v[228:231], v[72:75]\n\t" \
  "s_waitcnt lgkmcnt(7)\n\t" \
  "v_mfma_f32_16x16x32_bf16 v[48:51], v[120:123], v[224:227], v[48:51]\n\t" \
  "ds_read_b128 v[116:119], %[wa] offset:8192\n\t" \
  "v_mfma_f32_16x16x32_bf16 v[76:79], v[120:123], v[228:231], v[76:79]\n\t" \
  "v_mfma_f32_16x16x32_bf16 v[52:55], v[124:127], v[224:227], v[52:55]\n\t" \
  "ds_read_b128 v[120:123], %[wa] offset:9216\n\t" \
  "v_mfma_f32_16x16x32_bf16 v[80:83], v[124:127], v[228:231], v[80:83]\n\t" \
  "s_waitcnt lgkmcnt(7)\n\t" \
  "v_mfma_f32_16x16x32_bf16 v[56:59], v[128:131], v[224:227], v[56:59]\n\t" \
  "ds_read_b128 v[124:127], %[wa] offset:10240\n\t" \
  "v_mfma_f32_16x16x32_bf16 v[84:87], v[128:131], v[228:231], v[84:87]\n\t" \
  "v_mfma_f32_16x16x32_bf16 v[60:63], v[132:135], v[224:227], v[60:63]\n\t" \
  "ds_read_b128 v[128:131], %[wa] offset:11264\n\t" \
  "v_mfma_f32_16x16x32_bf16 v[88:91], v[132:135], v[228:231], v[88:91]\n\t" \
  "s_waitcnt lgkmcnt(8)\n\t" \
  "v_mfma_f32_16x16x32_bf16 v[64:67], v[136:139], v[224:227], v[64:67]\n\t" \
  "ds_read_b128 v[132:135], %[wa] offset:12288\n\t" \
  "v_mfma_f32_16x16x32_bf16 v[92:95], v[136:139], v[228:231], v[92:95]\n\t" \
  "s_waitcnt lgkmcnt(7)\n\t" \
  "v_mfma_f32_16x16x32_bf16 v[96:99], v[140:143], v[148:151], v[96:99]\n\t" \
  "ds_read_b128 v[136:139], %[wa] offset:13312\n\t" \
  "ds_read_b128 v[140:143], %[h0] offset:7168\n\t" \
  "v_mfma_f32_16x16x32_bf16 v[100:103], v[144:147], v[148:151], v[100:103]\n\t" \
  "ds_read_b128 v[144:147], %[h1] offset:7168\n\t" \
  "s_mov_b32 %[so], 15360\n\t" \
  "buffer_load_dwordx4 v[184:187], %[xb], %[rs], %[so] offen sc1\n\t" \
  "s_mov_b32 %[so], 33792\n\t" \
  "buffer_load_dwordx4 v[188:191], %[xb], %[rs], %[so] offen sc1\n\t" \
  "s_waitcnt vmcnt(12)\n\t" \
  "s_waitcnt lgkmcnt(7)\n\t" \
  "v_mfma_f32_16x16x32_bf16 v[40:43], v[112:115], v[232:235], v[40:43]\n\t" \
  "v_mfma_f32_16x16x32_bf16 v[68:71], v[112:115], v[236:239], v[68:71]\n\t" \
  "ds_read_b128 v[148:151], %[ha] offset:10240\n\t" \
  "v_mfma_f32_16x16x32_bf16 v[44:47], v[116:119], v[232:235], v[44:47]\n\t" \
  "ds_read_b128 v[112:115], %[wa] offset:14336\n\t" \
  "v_mfma_f32_16x16x32_bf16 v[72:75], v[116:119], v[236:239], v[72:75]\n\t" \
  "s_waitcnt lgkmcnt(7)\n\t" \
  "v_mfma_f32_16x16x32_bf16 v[48:51], v[120:123], v[232:235], v[48:51]\n\t" \
  "ds_read_b128 v[116:119], %[wa] offset:15360\n\t" \
  "v_mfma_f32_16x16x32_bf16 v[76:79], v[120:123], v[236:239], v[76:79]\n\t" \
  "v_mfma_f32_16x16x32_bf16 v[52:55], v[124:127], v[232:235], v[52:55]\n\t" \
  "ds_read_b128 v[120:123], %[wa] offset:16384\n\t" \
  "v_mfma_f32_16x16x32_bf16 v[80:83], v[124:127], v[236:239], v[80:83]\n\t" \
  "s_waitcnt lgkmcnt(7)\n\t" \
  "v_mfma_f32_16x16x32_bf16 v[56:59], v[128:131], v[232:235], v[56:59]\n\t" \
  "ds_read_b128 v[124:127], %[wa] offset:17408\n\t" \
  "v_mfma_f32_16x16x32_bf16 v[84:87], v[128:131], v[236:239], v[84:87]\n\t" \
  "v_mfma_f32_16x16x32_bf16 v[60:63], v[132:135], v[232:235], v[60:63]\n\t" \
  "ds_read_b128 v[128:131], %[wa] offset:18432\n\t" \
  "v_mfma_f32_16x16x32_bf16 v[88:91], v[132:135], v[236:239], v[88:91]\n\t" \
  "s_waitcnt lgkmcnt(8)\n\t" \
  "v_mfma_f32_16x16x32_bf16 v[64:67], v[136:139], v[232:235], v[64:67]\n\t" \
  "ds_read_b128 v[132:135], %[wa] offset:19456\n\t" \
  "v_mfma_f32_16x16x32_bf16 v[92:95], v[136:139], v[236:239], v[92:95]\n\t" \
  "s_waitcnt lgkmcnt(7)\n\t" \
  "v_mfma_f32_16x16x32_bf16 v[96:99], v[140:143], v[152:155], v[96:99]\n\t" \
  "ds_read_b128 v[136:139], %[wa] offset:20480\n\t" \
  "ds_read_b128 v[140:143], %[h0] offset:14336\n\t" \
  "v_mfma_f32_16x16x32_bf16 v[100:103], v[144:147], v[152:155], v[100:103]\n\t" \
  "ds_read_b128 v[144:147], %[h1] offset:14336\n\t" \
  "s_mov_b32 %[so], 16384\n\t" \
  "buffer_load_dwordx4 v[192:195], %[xb], %[rs], %[so] offen sc1\n\t" \
  "s_mov_b32 %[so], 34816\n\t" \
  "buffer_load_dwordx4 v[196:199], %[xb], %[rs], %[so] offen sc1\n\t" \
  "s_waitcnt vmcnt(12)\n\t" \
  "s_waitcnt lgkmcnt(7)\n\t" \
  "v_mfma_f32_16x16x32_bf16 v[40:43], v[112:115], v[240:243], v[40:43]\n\t" \
  "v_mfma_f32_16x16x32_bf16 v[68:71], v[112:115], v[244:247], v[68:71]\n\t" \
  "ds_read_b128 v[152:155], %[ha] offset:11264\n\t" \
  "v_mfma_f32_16x16x32_bf16 v[44:47], v[116:119], v[240:243], v[44:47]\n\t" \
  "ds_read_b128 v[112:115], %[wa] offset:21504\n\t" \
  "v_mfma_f32_16x16x32_bf16 v[72:75], v[116:119], v[244:247], v[72:75]\n\t" \
  "s_waitcnt lgkmcnt(7)\n\t" \
  "v_mfma_f32_16x16x32_bf16 v[48:51], v[120:123], v[240:243], v[48:51]\n\t" \
  "ds_read_b128 v[116:119], %[wa] offset:22528\n\t" \
  "v_mfma_f32_16x16x32_bf16 v[76:79], v[120:123], v[244:247], v[76:79]\n\t" \
  "v_mfma_f32_16x16x32_bf16 v[52:55], v[124:127], v[240:243], v[52:55]\n\t" \
  "ds_read_b128 v[120:123], %[wa] offset:23552\n\t" \
  "v_mfma_f32_16x16x32_bf16 v[80:83], v[124:127], v[244:247], v[80:83]\n\t" \
  "s_waitcnt lgkmcnt(7)\n\t" \
  "v_mfma_f32_16x16x32_bf16 v[56:59], v[128:131], v[240:243], v[56:59]\n\t" \
  "ds_read_b128 v[124:127], %[wa] offset:24576\n\t" \
  "v_mfma_f32_16x16x32_bf16 v[84:87], v[128:131], v[244:247], v[84:87]\n\t" \
  "v_mfma_f32_16x16x32_bf16 v[60:63], v[132:135], v[240:243], v[60:63]\n\t" \
  "ds_read_b128 v[128:131], %[wa] offset:25600\n\t" \
  "v_mfma_f32_16x16x32_bf16 v[88:91], v[132:135], v[244:247], v[88:91]\n\t" \
  "s_waitcnt lgkmcnt(8)\n\t" \
  "v_mfma_f32_16x16x32_bf16 v[64:67], v[136:139], v[240:243], v[64:67]\n\t" \
  "ds_read_b128 v[132:135], %[wa] offset:26624\n\t" \
  "v_mfma_f32_16x16x32_bf16 v[92:95], v[136:139], v[244:247], v[92:95]\n\t" \
  "s_waitcnt lgkmcnt(7)\n\t" \
  "v_mfma_f32_16x16x32_bf16 v[96:99], v[140:143], v[148:151], v[96:99]\n\t" \
  "ds_read_b128 v[136:139], %[wa] offset:27648\n\t" \
  "ds_read_b128 v[140:143], %[h0] offset:21504\n\t" \
  "v_mfma_f32_16x16x32_bf16 v[100:103], v[144:147], v[148:151], v[100:103]\n\t" \
  "ds_read_b128 v[144:147], %[h1] offset:21504\n\t" \
  "s_mov_b32 %[so], 17408\n\t" \
  "buffer_load_dwordx4 v[200:203], %[xb], %[rs], %[so] offen sc1\n\t" \
  "s_mov_b32 %[so], 35840\n\t" \
  "buffer_load_dwordx4 v[204:207], %[xb], %[rs], %[so] offen sc1\n\t" \
  "s_barrier\n\t" \
  "s_waitcnt vmcnt(12)\n\t" \
  "s_waitcnt lgkmcnt(7)\n\t" \
  "v_mfma_f32_16x16x32_bf16 v[40:43], v[112:115], v[248:251], v[40:43]\n\t" \
  "v_mfma_f32_16x16x32_bf16 v[68:71], v[112:115], v[252:255], v[68:71]\n\t" \
  "ds_read_b128 v[148:151], %[ha] offset:12288\n\t" \
  "v_mfma_f32_16x16x32_bf16 v[44:47], v[116:119], v[248:251], v[44:47]\n\t" \
  "ds_read_b128 v[112:115], %[wa] offset:28672\n\t" \
  "v_mfma_f32_16x16x32_bf16 v[72:75], v[116:119], v[252:255], v[72:75]\n\t" \
  "s_waitcnt lgkmcnt(7)\n\t" \
  "v_mfma_f32_16x16x32_bf16 v[48:51], v[120:123], v[248:251], v[48:51]\n\t" \
  "ds_read_b128 v[116:119], %[wa] offset:29696\n\t" \
  "v_mfma_f32_16x16x32_bf16 v[76:79], v[120:123], v[252:255], v[76:79]\n\t" \
  "v_mfma_f32_16x16x32_bf16 v[52:55], v[124:127], v[248:251], v[52:55]\n\t" \
  "ds_read_b128 v[120:123], %[wa] offset:30720\n\t" \
  "v_mfma_f32_16x16x32_bf16 v[80:83], v[124:127], v[252:255], v[80:83]\n\t" \
  "s_waitcnt lgkmcnt(7)\n\t" \
  "v_mfma_f32_16x16x32_bf16 v[56:59], v[128:131], v[248:251], v[56:59]\n\t" \
  "ds_read_b128 v[124:127], %[wa] offset:31744\n\t" \
  "v_mfma_f32_16x16x32_bf16 v[84:87], v[128:131], v[252:255], v[84:87]\n\t" \
  "v_mfma_f32_16x16x32_bf16 v[60:63], v[132:135], v[248:251], v[60:63]\n\t" \
  "ds_read_b128 v[128:131], %[wa] offset:32768\n\t" \
  "v_mfma_f32_16x16x32_bf16 v[88:91], v[132:135], v[252:255], v[88:91]\n\t" \
  "s_waitcnt lgkmcnt(8)\n\t" \
  "v_mfma_f32_16x16x32_bf16 v[64:67], v[136:139], v[248:251], v[64:67]\n\t" \
  "ds_read_b128 v[132:135], %[wa] offset:33792\n\t" \
  "v_mfma_f32_16x16x32_bf16 v[92:95], v[136:139], v[252:255], v[92:95]\n\t" \
  "s_waitcnt lgkmcnt(7)\n\t" \
  "v_mfma_f32_16x16x32_bf16 v[96:99], v[140:143], v[152:155], v[96:99]\n\t" \
  "ds_read_b128 v[136:139], %[wa] offset:34816\n\t" \
  "ds_read_b128 v[140:143], %[h0] offset:28672\n\t" \
  "v_mfma_f32_16x16x32_bf16 v[100:103], v[144:147], v[152:155], v[100:103]\n\t" \
  "ds_read_b128 v[144:147], %[h1] offset:28672\n\t" \
  "s_waitcnt vmcnt(10)\n\t" \
  "s_waitcnt lgkmcnt(7)\n\t" \
  "v_mfma_f32_16x16x32_bf16 v[40:43], v[112:115], v[160:163], v[40:43]\n\t" \
  "v_mfma_f32_16x16x32_bf16 v[68:71], v[112:115], v[164:167], v[68:71]\n\t" \
  "ds_read_b128 v[152:155], %[ha] offset:13312\n\t" \
  "v_mfma_f32_16x16x32_bf16 v[44:47], v[116:119], v[160:163], v[44:47]\n\t" \
  "ds_read_b128 v[112:115], %[wa] offset:35840\n\t" \
  "v_mfma_f32_16x16x32_bf16 v[72:75], v[116:119], v[164:167], v[72:75]\n\t" \
  "s_waitcnt lgkmcnt(7)\n\t" \
  "v_mfma_f32_16x16x32_bf16 v[48:51], v[120:123], v[160:163], v[48:51]\n\t" \
  "ds_read_b128 v[116:119], %[wa] offset:36864\n\t" \
  "v_mfma_f32_16x16x32_bf16 v[76:79], v[120:123], v[164:167], v[76:79]\n\t" \
  "v_mfma_f32_16x16x32_bf16 v[52:55], v[124:127], v[160:163], v[52:55]\n\t" \
  "ds_read_b128 v[120:123], %[wa] offset:37888\n\t" \
  "v_mfma_f32_16x16x32_bf16 v[80:83], v[124:127], v[164:167], v[80:83]\n\t" \
  "s_waitcnt lgkmcnt(7)\n\t" \
  "v_mfma_f32_16x16x32_bf16 v[56:59], v[128:131], v[160:163], v[56:59]\n\t" \
  "ds_read_b128 v[124:127], %[wa] offset:38912\n\t" \
  "v_mfma_f32_16x16x32_bf16 v[84:87], v[128:131], v[164:167], v[84:87]\n\t" \
  "v_mfma_f32_16x16x32_bf16 v[60:63], v[132:135], v[160:163], v[60:63]\n\t" \
  "ds_read_b128 v[128:131], %[wa] offset:39936\n\t" \
  "v_mfma_f32_16x16x32_bf16 v[88:91], v[132:135], v[164:167], v[88:91]\n\t" \
  "s_waitcnt lgkmcnt(8)\n\t" \
  "v_mfma_f32_16x16x32_bf16 v[64:67], v[136:139], v[160:163], v[64:67]\n\t" \
  "ds_read_b128 v[132:135], %[wa] offset:40960\n\t" \
  "v_mfma_f32_16x16x32_bf16 v[92:95], v[136:139], v[164:167], v[92:95]\n\t" \
  "s_waitcnt lgkmcnt(7)\n\t" \
  "v_mfma_f32_16x16x32_bf16 v[96:99], v[140:143], v[148:151], v[96:99]\n\t" \
  "ds_read_b128 v[136:139], %[wa] offset:41984\n\t" \
  "ds_read_b128 v[140:143], %[h0] offset:35840\n\t" \
  "v_mfma_f32_16x16x32_bf16 v[100:103], v[144:147], v[148:151], v[100:103]\n\t" \
  "ds_read_b128 v[144:147], %[h1] offset:35840\n\t" \
  "s_waitcnt vmcnt(8)\n\t" \
  "s_waitcnt lgkmcnt(7)\n\t" \
  "v_mfma_f32_16x16x32_bf16 v[40:43], v[112:115], v[168:171], v[40:43]\n\t" \
  "v_mfma_f32_16x16x32_bf16 v[68:71], v[112:115], v[172:175], v[68:71]\n\t" \
  "ds_read_b128 v[148:151], %[ha] offset:14336\n\t" \
  "v_mfma_f32_16x16x32_bf16 v[44:47], v[116:119], v[168:171], v[44:47]\n\t" \
  "ds_read_b128 v[112:115], %[wa] offset:43008\n\t" \
  "v_mfma_f32_16x16x32_bf16 v[72:75], v[116:119], v[172:175], v[72:75]\n\t" \
  "s_waitcnt lgkmcnt(7)\n\t" \
  "v_mfma_f32_16x16x32_bf16 v[48:51], v[120:123], v[168:171], v[48:51]\n\t" \
  "ds_read_b128 v[116:119], %[wa] offset:44032\n\t" \
  "v_mfma_f32_16x16x32_bf16 v[76:79], v[120:123], v[172:175], v[76:79]\n\t" \
  "v_mfma_f32_16x16x32_bf16 v[52:55], v[124:127], v[168:171], v[52:55]\n\t" \
  "ds_read_b128 v[120:123], %[wa] offset:45056\n\t" \
  "v_mfma_f32_16x16x32_bf16 v[80:83], v[124:127], v[172:175], v[80:83]\n\t" \
  "s_waitcnt lgkmcnt(7)\n\t" \
  "v_mfma_f32_16x16x32_bf16 v[56:59], v[128:131], v[168:171], v[56:59]\n\t" \
  "ds_read_b128 v[124:127], %[wa] offset:46080\n\t" \
  "v_mfma_f32_16x16x32_bf16 v[84:87], v[128:131], v[172:175], v[84:87]\n\t" \
  "v_mfma_f32_16x16x32_bf16 v[60:63], v[132:135], v[168:171], v[60:63]\n\t" \
  "ds_read_b128 v[128:131], %[wa] offset:47104\n\t" \
  "v_mfma_f32_16x16x32_bf16 v[88:91], v[132:135], v[172:175], v[88:91]\n\t" \
  "s_waitcnt lgkmcnt(8)\n\t" \
  "v_mfma_f32_16x16x32_bf16 v[64:67], v[136:139], v[168:171], v[64:67]\n\t" \
  "ds_read_b128 v[132:135], %[wa] offset:48128\n\t" \
  "v_mfma_f32_16x16x32_bf16 v[92:95], v[136:139], v[172:175], v[92:95]\n\t" \
  "s_waitcnt lgkmcnt(7)\n\t" \
  "v_mfma_f32_16x16x32_bf16 v[96:99], v[140:143], v[152:155], v[96:99]\n\t" \
  "ds_read_b128 v[136:139], %[wa] offset:49152\n\t" \
  "ds_read_b128 v[140:143], %[h0] offset:43008\n\t" \
  "v_mfma_f32_16x16x32_bf16 v[100:103], v[144:147], v[152:155], v[100:103]\n\t" \
  "ds_read_b128 v[144:147], %[h1] offset:43008\n\t" \
  "s_barrier\n\t" \
  "s_waitcnt vmcnt(6)\n\t" \
  "s_waitcnt lgkmcnt(7)\n\t" \
  "v_mfma_f32_16x16x32_bf16 v[40:43], v[112:115], v[176:179], v[40:43]\n\t" \
  "v_mfma_f32_16x16x32_bf16 v[68:71], v[112:115], v[180:183], v[68:71]\n\t" \
  "ds_read_b128 v[152:155], %[ha] offset:15360\n\t" \
  "v_mfma_f32_16x16x32_bf16 v[44:47], v[116:119], v[176:179], v[44:47]\n\t" \
  "ds_read_b128 v[112:115], %[wa] offset:50176\n\t" \
  "v_mfma_f32_16x16x32_bf16 v[72:75], v[116:119], v[180:183], v[72:75]\n\t" \
  "s_waitcnt lgkmcnt(7)\n\t" \
  "v_mfma_f32_16x16x32_bf16 v[48:51], v[120:123], v[176:179], v[48:51]\n\t" \
  "ds_read_b128 v[116:119], %[wa] offset:51200\n\t" \
  "v_mfma_f32_16x16x32_bf16 v[76:79], v[120:123], v[180:183], v[76:79]\n\t" \
  "v_mfma_f32_16x16x32_bf16 v[52:55], v[124:127], v[176:179], v[52:55]\n\t" \
  "ds_read_b128 v[120:123], %[wa] offset:52224\n\t" \
  "v_mfma_f32_16x16x32_bf16 v[80:83], v[124:127], v[180:183], v[80:83]\n\t" \
  "s_waitcnt lgkmcnt(7)\n\t" \
  "v_mfma_f32_16x16x32_bf16 v[56:59], v[128:131], v[176:179], v[56:59]\n\t" \
  "ds_read_b128 v[124:127], %[wa] offset:53248\n\t" \
  "v_mfma_f32_16x16x32_bf16 v[84:87], v[128:131], v[180:183], v[84:87]\n\t" \
  "v_mfma_f32_16x16x32_bf16 v[60:63], v[132:135], v[176:179], v[60:63]\n\t" \
  "ds_read_b128 v[128:131], %[wa] offset:54272\n\t" \
  "v_mfma_f32_16x16x32_bf16 v[88:91], v[132:135], v[180:183], v[88:91]\n\t" \
  "s_waitcnt lgkmcnt(8)\n\t" \
  "v_mfma_f32_16x16x32_bf16 v[64:67], v[136:139], v[176:179], v[64:67]\n\t" \
  "ds_read_b128 v[132:135], %[wa] offset:55296\n\t" \
  "v_mfma_f32_16x16x32_bf16 v[92:95], v[136:139], v[180:183], v[92:95]\n\t" \
  "s_waitcnt lgkmcnt(7)\n\t" \
  "v_mfma_f32_16x16x32_bf16 v[96:99], v[140:143], v[148:151], v[96:99]\n\t" \
  "ds_read_b128 v[136:139], %[wa] offset:56320\n\t" \
  "ds_read_b128 v[140:143], %[h0] offset:50176\n\t" \
  "v_mfma_f32_16x16x32_bf16 v[100:103], v[144:147], v[148:151], v[100:103]\n\t" \
  "ds_read_b128 v[144:147], %[h1] offset:50176\n\t" \
  "v_add_u32 %[wa], 57344, %[wa]\n\t" \
  "v_add_u32 %[h0], 57344, %[h0]\n\t" \
  "v_add_u32 %[h1], 57344, %[h1]\n\t" \
  "s_waitcnt vmcnt(4)\n\t" \
  "s_waitcnt lgkmcnt(7)\n\t" \
  "v_mfma_f32_16x16x32_bf16 v[40:43], v[112:115], v[184:187], v[40:43]\n\t" \
  "v_mfma_f32_16x16x32_bf16 v[68:71], v[112:115], v[188:191], v[68:71]\n\t" \
  "ds_read_b128 v[148:151], %[ha] offset:16384\n\t" \
  "v_mfma_f32_16x16x32_bf16 v[44:47], v[116:119], v[184:187], v[44:47]\n\t" \
  "ds_read_b128 v[112:115], %[wa] offset:0\n\t" \
  "v_mfma_f32_16x16x32_bf16 v[72:75], v[116:119], v[188:191], v[72:75]\n\t" \
  "s_waitcnt lgkmcnt(7)\n\t" \
  "v_mfma_f32_16x16x32_bf16 v[48:51], v[120:123], v[184:187], v[48:51]\n\t" \
  "ds_read_b128 v[116:119], %[wa] offset:1024\n\t" \
  "v_mfma_f32_16x16x32_bf16 v[76:79], v[120:123], v[188:191], v[76:79]\n\t" \
  "v_mfma_f32_16x16x32_bf16 v[52:55], v[124:127], v[184:187], v[52:55]\n\t" \
  "ds_read_b128 v[120:123], %[wa] offset:2048\n\t" \
  "v_mfma_f32_16x16x32_bf16 v[80:83], v[124:127], v[188:191], v[80:83]\n\t" \
  "s_waitcnt lgkmcnt(7)\n\t" \
  "v_mfma_f32_16x16x32_bf16 v[56:59], v[128:131], v[184:187], v[56:59]\n\t" \
  "ds_read_b128 v[124:127], %[wa] offset:3072\n\t" \
  "v_mfma_f32_16x16x32_bf16 v[84:87], v[128:131], v[188:191], v[84:87]\n\t" \
  "v_mfma_f32_16x16x32_bf16 v[60:63], v[132:135], v[184:187], v[60:63]\n\t" \
  "ds_read_b128 v[128:131], %[wa] offset:4096\n\t" \
  "v_mfma_f32_16x16x32_bf16 v[88:91], v[132:135], v[188:191], v[88:91]\n\t" \
  "s_waitcnt lgkmcnt(8)\n\t" \
  "v_mfma_f32_16x16x32_bf16 v[64:67], v[136:139], v[184:187], v[64:67]\n\t" \
  "ds_read_b128 v[132:135], %[wa] offset:5120\n\t" \
  "v_mfma_f32_16x16x32_bf16 v[92:95], v[136:139], v[188:191], v[92:95]\n\t" \
  "s_waitcnt lgkmcnt(7)\n\t" \
  "v_mfma_f32_16x16x32_bf16 v[96:99], v[140:143], v[152:155], v[96:99]\n\t" \
  "ds_read_b128 v[136:139], %[wa] offset:6144\n\t" \
  "ds_read_b128 v[140:143], %[h0] offset:0\n\t" \
  "v_mfma_f32_16x16x32_bf16 v[100:103], v[144:147], v[152:155], v[100:103]\n\t" \
  "ds_read_b128 v[144:147], %[h1] offset:0\n\t" \
  "s_waitcnt vmcnt(2)\n\t" \
  "s_waitcnt lgkmcnt(7)\n\t" \
  "v_mfma_f32_16x16x32_bf16 v[40:43], v[112:115], v[192:195], v[40:43]\n\t" \
  "v_mfma_f32_16x16x32_bf16 v[68:71], v[112:115], v[196:199], v[68:71]\n\t" \
  "ds_read_b128 v[152:155], %[ha] offset:17408\n\t" \
  "v_mfma_f32_16x16x32_bf16 v[44:47], v[116:119], v[192:195], v[44:47]\n\t" \
  "ds_read_b128 v[112:115], %[wa] offset:7168\n\t" \
  "v_mfma_f32_16x16x32_bf16 v[72:75], v[116:119], v[196:199], v[72:75]\n\t" \
  "s_waitcnt lgkmcnt(7)\n\t" \
  "v_mfma_f32_16x16x32_bf16 v[48:51], v[120:123], v[192:195], v[48:51]\n\t" \
  "ds_read_b128 v[116:119], %[wa] offset:8192\n\t" \
  "v_mfma_f32_16x16x32_bf16 v[76:79], v[120:123], v[196:199], v[76:79]\n\t" \
  "v_mfma_f32_16x16x32_bf16 v[52:55], v[124:127], v[192:195], v[52:55]\n\t" \
  "ds_read_b128 v[120:123], %[wa] offset:9216\n\t" \
  "v_mfma_f32_16x16x32_bf16 v[80:83], v[124:127], v[196:199], v[80:83]\n\t" \
  "s_waitcnt lgkmcnt(7)\n\t" \
  "v_mfma_f32_16x16x32_bf16 v[56:59], v[128:131], v[192:195], v[56:59]\n\t" \
  "ds_read_b128 v[124:127], %[wa] offset:10240\n\t" \
  "v_mfma_f32_16x16x32_bf16 v[84:87], v[128:131], v[196:199], v[84:87]\n\t" \
  "v_mfma_f32_16x16x32_bf16 v[60:63], v[132:135], v[192:195], v[60:63]\n\t" \
  "ds_read_b128 v[128:131], %[wa] offset:11264\n\t" \
  "v_mfma_f32_16x16x32_bf16 v[88:91], v[132:135], v[196:199], v[88:91]\n\t" \
  "s_waitcnt lgkmcnt(8)\n\t" \
  "v_mfma_f32_16x16x32_bf16 v[64:67], v[136:139], v[192:195], v[64:67]\n\t" \
  "ds_read_b128 v[132:135], %[wa] offset:12288\n\t" \
  "v_mfma_f32_16x16x32_bf16 v[92:95], v[136:139], v[196:199], v[92:95]\n\t" \
  "s_waitcnt lgkmcnt(7)\n\t" \
  "v_mfma_f32_16x16x32_bf16 v[96:99], v[140:143], v[148:151], v[96:99]\n\t" \
  "ds_read_b128 v[136:139], %[wa] offset:13312\n\t" \
  "ds_read_b128 v[140:143], %[h0] offset:7168\n\t" \
  "v_mfma_f32_16x16x32_bf16 v[100:103], v[144:147], v[148:151], v[100:103]\n\t" \
  "ds_read_b128 v[144:147], %[h1] offset:7168\n\t" \
  "s_waitcnt vmcnt(0)\n\t" \
  "s_waitcnt lgkmcnt(7)\n\t" \
  "v_mfma_f32_16x16x32_bf16 v[40:43], v[112:115], v[200:203], v[40:43]\n\t" \
  "v_mfma_f32_16x16x32_bf16 v[68:71], v[112:115], v[204:207], v[68:71]\n\t" \
  "v_mfma_f32_16x16x32_bf16 v[44:47], v[116:119], v[200:203], v[44:47]\n\t" \
  "v_mfma_f32_16x16x32_bf16 v[72:75], v[116:119], v[204:207], v[72:75]\n\t" \
  "s_waitcnt lgkmcnt(5)\n\t" \
  "v_mfma_f32_16x16x32_bf16 v[48:51], v[120:123], v[200:203], v[48:51]\n\t" \
  "v_mfma_f32_16x16x32_bf16 v[76:79], v[120:123], v[204:207], v[76:79]\n\t" \
  "v_mfma_f32_16x16x32_bf16 v[52:55], v[124:127], v[200:203], v[52:55]\n\t" \
  "v_mfma_f32_16x16x32_bf16 v[80:83], v[124:127], v[204:207], v[80:83]\n\t" \
  "s_waitcnt lgkmcnt(3)\n\t" \
  "v_mfma_f32_16x16x32_bf16 v[56:59], v[128:131], v[200:203], v[56:59]\n\t" \
  "v_mfma_f32_16x16x32_bf16 v[84:87], v[128:131], v[204:207], v[84:87]\n\t" \
  "v_mfma_f32_16x16x32_bf16 v[60:63], v[132:135], v[200:203], v[60:63]\n\t" \
  "v_mfma_f32_16x16x32_bf16 v[88:91], v[132:135], v[204:207], v[88:91]\n\t" \
  "s_waitcnt lgkmcnt(2)\n\t" \
  "v_mfma_f32_16x16x32_bf16 v[64:67], v[136:139], v[200:203], v[64:67]\n\t" \
  "v_mfma_f32_16x16x32_bf16 v[92:95], v[136:139], v[204:207], v[92:95]\n\t" \
  "s_waitcnt lgkmcnt(0)\n\t" \
  "v_mfma_f32_16x16x32_bf16 v[96:99], v[140:143], v[152:155], v[96:99]\n\t" \
  "v_mfma_f32_16x16x32_bf16 v[100:103], v[144:147], v[152:155], v[100:103]\n\t" \
  "s_nop 7\n\t" \
  "s_nop 7\n\t"
#define A_LOOP_CLOB "v112", "v113", "v114", "v115", "v116", "v117", "v118", "v119", "v120", "v121", "v122", "v123", "v124", "v125", "v126", "v127", "v128", "v129", "v130", "v131", "v132", "v133", "v134", "v135", "v136", "v137", "v138", "v139", "v140", "v141", "v142", "v143", "v144", "v145", "v146", "v147", "v148", "v149", "v150", "v151", "v152", "v153", "v154", "v155", "v156", "v157", "v158", "v159", "v160", "v161", "v162", "v163", "v164", "v165", "v166", "v167", "v168", "v169", "v170", "v171", "v172", "v173", "v174", "v175", "v176", "v177", "v178", "v179", "v180", "v181", "v182", "v183", "v184", "v185", "v186", "v187", "v188", "v189", "v190", "v191", "v192", "v193", "v194", "v195", "v196", "v197", "v198", "v199", "v200", "v201", "v202", "v203", "v204", "v205", "v206", "v207", "v208", "v209", "v210", "v211", "v212", "v213", "v214", "v215", "v216", "v217", "v218", "v219", "v220", "v221", "v222", "v223", "v224", "v225", "v226", "v227", "v228", "v229", "v230", "v231", "v232", "v233", "v234", "v235", "v236", "v237", "v238", "v239", "v240", "v241", "v242", "v243", "v244", "v245", "v246", "v247", "v248", "v249", "v250", "v251", "v252", "v253", "v254", "v255"
#define WO_LOOP_ASM \
  "s_nop 4\n\t" \
  "s_mov_b32 %[so], 0\n\t" \
  "buffer_load_dwordx4 v[108:111], %[xb], %[rs], %[so] offen sc1\n\t" \
  "s_mov_b32 %[so], 1024\n\t" \
  "buffer_load_dwordx4 v[112:115], %[xb], %[rs], %[so] offen sc1\n\t" \
  "s_mov_b32 %[so], 2048\n\t" \
  "buffer_load_dwordx4 v[116:119], %[xb], %[rs], %[so] offen sc1\n\t" \
  "s_mov_b32 %[so], 3072\n\t" \
  "buffer_load_dwordx4 v[120:123], %[xb], %[rs], %[so] offen sc1\n\t" \
  "s_mov_b32 %[so], 4096\n\t" \
  "buffer_load_dwordx4 v[124:127], %[xb], %[rs], %[so] offen sc1\n\t" \
  "s_mov_b32 %[so], 5120\n\t" \
  "buffer_load_dwordx4 v[128:131], %[xb], %[rs], %[so] offen sc1\n\t" \
  "s_mov_b32 %[so], 6144\n\t" \
  "buffer_load_dwordx4 v[132:135], %[xb], %[rs], %[so] offen sc1\n\t" \
  "s_mov_b32 %[so], 7168\n\t" \
  "buffer_load_dwordx4 v[136:139], %[xb], %[rs], %[so] offen sc1\n\t" \
  "s_mov_b32 %[so], 8192\n\t" \
  "buffer_load_dwordx4 v[140:143], %[xb], %[rs], %[so] offen sc1\n\t" \
  "s_mov_b32 %[so], 9216\n\t" \
  "buffer_load_dwordx4 v[144:147], %[xb], %[rs], %[so] offen sc1\n\t" \
  "s_waitcnt vmcnt(10)\n\t" \
  "s_barrier\n\t" \
  "ds_read_b128 v[68:71], %[wa] offset:0\n\t" \
  "ds_read_b128 v[72:75], %[wa] offset:1024\n\t" \
  "ds_read_b128 v[76:79], %[wa] offset:2048\n\t" \
  "ds_read_b128 v[80:83], %[wa] offset:3072\n\t" \
  "ds_read_b128 v[84:87], %[wa] offset:4096\n\t" \
  "s_waitcnt vmcnt(9)\n\t" \
  "s_waitcnt lgkmcnt(2)\n\t" \
  "v_mfma_f32_16x16x32_bf16 v[40:43], v[68:71], v[108:111], 0\n\t" \
  "ds_read_b128 v[88:91], %[wa] offset:5120\n\t" \
  "v_mfma_f32_16x16x32_bf16 v[44:47], v[72:75], v[108:111], 0\n\t" \
  "ds_read_b128 v[92:95], %[wa] offset:6144\n\t" \
  "v_mfma_f32_16x16x32_bf16 v[48:51], v[76:79], v[108:111], 0\n\t" \
  "ds_read_b128 v[96:99], %[wa] offset:7168\n\t" \
  "s_waitcnt lgkmcnt(3)\n\t" \
  "v_mfma_f32_16x16x32_bf16 v[52:55], v[80:83], v[108:111], 0\n\t" \
  "ds_read_b128 v[100:103], %[wa] offset:8192\n\t" \
  "v_mfma_f32_16x16x32_bf16 v[56:59], v[84:87], v[108:111], 0\n\t" \
  "ds_read_b128 v[104:107], %[wa] offset:9216\n\t" \
  "s_mov_b32 %[so], 10240\n\t" \
  "buffer_load_dwordx4 v[148:151], %[xb], %[rs], %[so] offen sc1\n\t" \
  "s_mov_b32 %[so], 11264\n\t" \
  "buffer_load_dwordx4 v[152:155], %[xb], %[rs], %[so] offen sc1\n\t" \
  "s_waitcnt vmcnt(10)\n\t" \
  "s_waitcnt lgkmcnt(2)\n\t" \
  "v_mfma_f32_16x16x32_bf16 v[40:43], v[88:91], v[112:115], v[40:43]\n\t" \
  "ds_read_b128 v[68:71], %[wa] offset:10240\n\t" \
  "v_mfma_f32_16x16x32_bf16 v[44:47], v[92:95], v[112:115], v[44:47]\n\t" \
  "ds_read_b128 v[72:75], %[wa] offset:11264\n\t" \
  "v_mfma_f32_16x16x32_bf16 v[48:51], v[96:99], v[112:115], v[48:51]\n\t" \
  "ds_read_b128 v[76:79], %[wa] offset:12288\n\t" \
  "s_waitcnt lgkmcnt(3)\n\t" \
  "v_mfma_f32_16x16x32_bf16 v[52:55], v[100:103], v[112:115], v[52:55]\n\t" \
  "ds_read_b128 v[80:83], %[wa] offset:13312\n\t" \
  "v_mfma_f32_16x16x32_bf16 v[56:59], v[104:107], v[112:115], v[56:59]\n\t" \
  "ds_read_b128 v[84:87], %[wa] offset:14336\n\t" \
  "s_mov_b32 %[so], 12288\n\t" \
  "buffer_load_dwordx4 v[156:159], %[xb], %[rs], %[so] offen sc1\n\t" \
  "s_mov_b32 %[so], 13312\n\t" \
  "buffer_load_dwordx4 v[160:163], %[xb], %[rs], %[so] offen sc1\n\t" \
  "s_waitcnt vmcnt(11)\n\t" \
  "s_waitcnt lgkmcnt(2)\n\t" \
  "v_mfma_f32_16x16x32_bf16 v[40:43], v[68:71], v[116:119], v[40:43]\n\t" \
  "ds_read_b128 v[88:91], %[wa] offset:15360\n\t" \
  "v_mfma_f32_16x16x32_bf16 v[44:47], v[72:75], v[116:119], v[44:47]\n\t" \
  "ds_read_b128 v[92:95], %[wa] offset:16384\n\t" \
  "v_mfma_f32_16x16x32_bf16 v[48:51], v[76:79], v[116:119], v[48:51]\n\t" \
  "ds_read_b128 v[96:99], %[wa] offset:17408\n\t" \
  "s_waitcnt lgkmcnt(3)\n\t" \
  "v_mfma_f32_16x16x32_bf16 v[52:55], v[80:83], v[116:119], v[52:55]\n\t" \
  "ds_read_b128 v[100:103], %[wa] offset:18432\n\t" \
  "v_mfma_f32_16x16x32_bf16 v[56:59], v[84:87], v[116:119], v[56:59]\n\t" \
  "ds_read_b128 v[104:107], %[wa] offset:19456\n\t" \
  "s_mov_b32 %[so], 14336\n\t" \
  "buffer_load_dwordx4 v[164:167], %[xb], %[rs], %[so] offen sc1\n\t" \
  "s_mov_b32 %[so], 15360\n\t" \
  "buffer_load_dwordx4 v[168:171], %[xb], %[rs], %[so] offen sc1\n\t" \
  "s_waitcnt vmcnt(12)\n\t" \
  "s_waitcnt lgkmcnt(2)\n\t" \
  "v_mfma_f32_16x16x32_bf16 v[40:43], v[88:91], v[120:123], v[40:43]\n\t" \
  "ds_read_b128 v[68:71], %[wa] offset:20480\n\t" \
  "v_mfma_f32_16x16x32_bf16 v[44:47], v[92:95], v[120:123], v[44:47]\n\t" \
  "ds_read_b128 v[72:75], %[wa] offset:21504\n\t" \
  "v_mfma_f32_16x16x32_bf16 v[48:51], v[96:99], v[120:123], v[48:51]\n\t" \
  "ds_read_b128 v[76:79], %[wa] offset:22528\n\t" \
  "s_waitcnt lgkmcnt(3)\n\t" \
  "v_mfma_f32_16x16x32_bf16 v[52:55], v[100:103], v[120:123], v[52:55]\n\t" \
  "ds_read_b128 v[80:83], %[wa] offset:23552\n\t" \
  "v_mfma_f32_16x16x32_bf16 v[56:59], v[104:107], v[120:123], v[56:59]\n\t" \
  "ds_read_b128 v[84:87], %[wa] offset:24576\n\t" \
  "s_mov_b32 %[so], 16384\n\t" \
  "buffer_load_dwordx4 v[172:175], %[xb], %[rs], %[so] offen sc1\n\t" \
  "s_mov_b32 %[so], 17408\n\t" \
  "buffer_load_dwordx4 v[176:179], %[xb], %[rs], %[so] offen sc1\n\t" \
  "s_waitcnt vmcnt(13)\n\t" \
  "s_waitcnt lgkmcnt(2)\n\t" \
  "v_mfma_f32_16x16x32_bf16 v[40:43], v[68:71], v[124:127], v[40:43]\n\t" \
  "ds_read_b128 v[88:91], %[wa] offset:25600\n\t" \
  "v_mfma_f32_16x16x32_bf16 v[44:47], v[72:75], v[124:127], v[44:47]\n\t" \
  "ds_read_b128 v[92:95], %[wa] offset:26624\n\t" \
  "v_mfma_f32_16x16x32_bf16 v[48:51], v[76:79], v[124:127], v[48:51]\n\t" \
  "ds_read_b128 v[96:99], %[wa] offset:27648\n\t" \
  "s_waitcnt lgkmcnt(3)\n\t" \
  "v_mfma_f32_16x16x32_bf16 v[52:55], v[80:83], v[124:127], v[52:55]\n\t" \
  "ds_read_b128 v[100:103], %[wa] offset:28672\n\t" \
  "v_mfma_f32_16x16x32_bf16 v[56:59], v[84:87], v[124:127], v[56:59]\n\t" \
  "ds_read_b128 v[104:107], %[wa] offset:29696\n\t" \
  "s_waitcnt vmcnt(12)\n\t" \
  "s_waitcnt lgkmcnt(2)\n\t" \
  "v_mfma_f32_16x16x32_bf16 v[40:43], v[88:91], v[128:131], v[40:43]\n\t" \
  "ds_read_b128 v[68:71], %[wa] offset:30720\n\t" \
  "v_mfma_f32_16x16x32_bf16 v[44:47], v[92:95], v[128:131], v[44:47]\n\t" \
  "ds_read_b128 v[72:75], %[wa] offset:31744\n\t" \
  "v_mfma_f32_16x16x32_bf16 v[48:51], v[96:99], v[128:131], v[48:51]\n\t" \
  "ds_read_b128 v[76:79], %[wa] offset:32768\n\t" \
  "s_waitcnt lgkmcnt(3)\n\t" \
  "v_mfma_f32_16x16x32_bf16 v[52:55], v[100:103], v[128:131], v[52:55]\n\t" \
  "ds_read_b128 v[80:83], %[wa] offset:33792\n\t" \
  "v_mfma_f32_16x16x32_bf16 v[56:59], v[104:107], v[128:131], v[56:59]\n\t" \
  "ds_read_b128 v[84:87], %[wa] offset:34816\n\t" \
  "s_waitcnt vmcnt(11)\n\t" \
  "s_waitcnt lgkmcnt(2)\n\t" \
  "v_mfma_f32_16x16x32_bf16 v[40:43], v[68:71], v[132:135], v[40:43]\n\t" \
  "ds_read_b128 v[88:91], %[wa] offset:35840\n\t" \
  "v_mfma_f32_16x16x32_bf16 v[44:47], v[72:75], v[132:135], v[44:47]\n\t" \
  "ds_read_b128 v[92:95], %[wa] offset:36864\n\t" \
  "v_mfma_f32_16x16x32_bf16 v[48:51], v[76:79], v[132:135], v[48:51]\n\t" \
  "ds_read_b128 v[96:99], %[wa] offset:37888\n\t" \
  "s_waitcnt lgkmcnt(3)\n\t" \
  "v_mfma_f32_16x16x32_bf16 v[52:55], v[80:83], v[132:135], v[52:55]\n\t" \
  "ds_read_b128 v[100:103], %[wa] offset:38912\n\t" \
  "v_mfma_f32_16x16x32_bf16 v[56:59], v[84:87], v[132:135], v[56:59]\n\t" \
  "ds_read_b128 v[104:107], %[wa] offset:39936\n\t" \
  "s_waitcnt vmcnt(10)\n\t" \
  "s_waitcnt lgkmcnt(2)\n\t" \
  "v_mfma_f32_16x16x32_bf16 v[40:43], v[88:91], v[136:139], v[40:43]\n\t" \
  "ds_read_b128 v[68:71], %[wa] offset:40960\n\t" \
  "v_mfma_f32_16x16x32_bf16 v[44:47], v[92:95], v[136:139], v[44:47]\n\t" \
  "ds_read_b128 v[72:75], %[wa] offset:41984\n\t" \
  "v_mfma_f32_16x16x32_bf16 v[48:51], v[96:99], v[136:139], v[48:51]\n\t" \
  "ds_read_b128 v[76:79], %[wa] offset:43008\n\t" \
  "s_waitcnt lgkmcnt(3)\n\t" \
  "v_mfma_f32_16x16x32_bf16 v[52:55], v[100:103], v[136:139], v[52:55]\n\t" \
  "ds_read_b128 v[80:83], %[wa] offset:44032\n\t" \
  "v_mfma_f32_16x16x32_bf16 v[56:59], v[104:107], v[136:139], v[56:59]\n\t" \
  "ds_read_b128 v[84:87], %[wa] offset:45056\n\t" \
  "s_waitcnt vmcnt(9)\n\t" \
  "s_waitcnt lgkmcnt(2)\n\t" \
  "v_mfma_f32_16x16x32_bf16 v[40:43], v[68:71], v[140:143], v[40:43]\n\t" \
  "ds_read_b128 v[88:91], %[wa] offset:46080\n\t" \
  "v_mfma_f32_16x16x32_bf16 v[44:47], v[72:75], v[140:143], v[44:47]\n\t" \
  "ds_read_b128 v[92:95], %[wa] offset:47104\n\t" \
  "v_mfma_f32_16x16x32_bf16 v[48:51], v[76:79], v[140:143], v[48:51]\n\t" \
  "ds_read_b128 v[96:99], %[wa] offset:48128\n\t" \
  "s_waitcnt lgkmcnt(3)\n\t" \
  "v_mfma_f32_16x16x32_bf16 v[52:55], v[80:83], v[140:143], v[52:55]\n\t" \
  "ds_read_b128 v[100:103], %[wa] offset:49152\n\t" \
  "v_mfma_f32_16x16x32_bf16 v[56:59], v[84:87], v[140:143], v[56:59]\n\t" \
  "ds_read_b128 v[104:107], %[wa] offset:50176\n\t" \
  "s_waitcnt vmcnt(8)\n\t" \
  "s_waitcnt lgkmcnt(2)\n\t" \
  "v_mfma_f32_16x16x32_bf16 v[40:43], v[88:91], v[144:147], v[40:43]\n\t" \
  "ds_read_b128 v[68:71], %[wa] offset:51200\n\t" \
  "v_mfma_f32_16x16x32_bf16 v[44:47], v[92:95], v[144:147], v[44:47]\n\t" \
  "ds_read_b128 v[72:75], %[wa] offset:52224\n\t" \
  "v_mfma_f32_16x16x32_bf16 v[48:51], v[96:99], v[144:147], v[48:51]\n\t" \
  "ds_read_b128 v[76:79], %[wa] offset:53248\n\t" \
  "s_waitcnt lgkmcnt(3)\n\t" \
  "v_mfma_f32_16x16x32_bf16 v[52:55], v[100:103], v[144:147], v[52:55]\n\t" \
  "ds_read_b128 v[80:83], %[wa] offset:54272\n\t" \
  "v_mfma_f32_16x16x32_bf16 v[56:59], v[104:107], v[144:147], v[56:59]\n\t" \
  "ds_read_b128 v[84:87], %[wa] offset:55296\n\t" \
  "s_waitcnt vmcnt(7)\n\t" \
  "s_waitcnt lgkmcnt(2)\n\t" \
  "v_mfma_f32_16x16x32_bf16 v[40:43], v[68:71], v[148:151], v[40:43]\n\t" \
  "ds_read_b128 v[88:91], %[wa] offset:56320\n\t" \
  "v_mfma_f32_16x16x32_bf16 v[44:47], v[72:75], v[148:151], v[44:47]\n\t" \
  "ds_read_b128 v[92:95], %[wa] offset:57344\n\t" \
  "v_mfma_f32_16x16x32_bf16 v[48:51], v[76:79], v[148:151], v[48:51]\n\t" \
  "ds_read_b128 v[96:99], %[wa] offset:58368\n\t" \
  "s_waitcnt lgkmcnt(3)\n\t" \
  "v_mfma_f32_16x16x32_bf16 v[52:55], v[80:83], v[148:151], v[52:55]\n\t" \
  "ds_read_b128 v[100:103], %[wa] offset:59392\n\t" \
  "v_mfma_f32_16x16x32_bf16 v[56:59], v[84:87], v[148:151], v[56:59]\n\t" \
  "ds_read_b128 v[104:107], %[wa] offset:60416\n\t" \
  "v_add_u32 %[wa], 61440, %[wa]\n\t" \
  "s_waitcnt vmcnt(6)\n\t" \
  "s_waitcnt lgkmcnt(2)\n\t" \
  "v_mfma_f32_16x16x32_bf16 v[40:43], v[88:91], v[152:155], v[40:43]\n\t" \
  "ds_read_b128 v[68:71], %[wa] offset:0\n\t" \
  "v_mfma_f32_16x16x32_bf16 v[44:47], v[92:95], v[152:155], v[44:47]\n\t" \
  "ds_read_b128 v[72:75], %[wa] offset:1024\n\t" \
  "v_mfma_f32_16x16x32_bf16 v[48:51], v[96:99], v[152:155], v[48:51]\n\t" \
  "ds_read_b128 v[76:79], %[wa] offset:2048\n\t" \
  "s_waitcnt lgkmcnt(3)\n\t" \
  "v_mfma_f32_16x16x32_bf16 v[52:55], v[100:103], v[152:155], v[52:55]\n\t" \
  "ds_read_b128 v[80:83], %[wa] offset:3072\n\t" \
  "v_mfma_f32_16x16x32_bf16 v[56:59], v[104:107], v[152:155], v[56:59]\n\t" \
  "ds_read_b128 v[84:87], %[wa] offset:4096\n\t" \
  "s_waitcnt vmcnt(5)\n\t" \
  "s_waitcnt lgkmcnt(2)\n\t" \
  "v_mfma_f32_16x16x32_bf16 v[40:43], v[68:71], v[156:159], v[40:43]\n\t" \
  "ds_read_b128 v[88:91], %[wa] offset:5120\n\t" \
  "v_mfma_f32_16x16x32_bf16 v[44:47], v[72:75], v[156:159], v[44:47]\n\t" \
  "ds_read_b128 v[92:95], %[wa] offset:6144\n\t" \
  "v_mfma_f32_16x16x32_bf16 v[48:51], v[76:79], v[156:159], v[48:51]\n\t" \
  "ds_read_b128 v[96:99], %[wa] offset:7168\n\t" \
  "s_waitcnt lgkmcnt(3)\n\t" \
  "v_mfma_f32_16x16x32_bf16 v[52:55], v[80:83], v[156:159], v[52:55]\n\t" \
  "ds_read_b128 v[100:103], %[wa] offset:8192\n\t" \
  "v_mfma_f32_16x16x32_bf16 v[56:59], v[84:87], v[156:159], v[56:59]\n\t" \
  "ds_read_b128 v[104:107], %[wa] offset:9216\n\t" \
  "s_waitcnt vmcnt(4)\n\t" \
  "s_waitcnt lgkmcnt(2)\n\t" \
  "v_mfma_f32_16x16x32_bf16 v[40:43], v[88:91], v[160:163], v[40:43]\n\t" \
  "ds_read_b128 v[68:71], %[wa] offset:10240\n\t" \
  "v_mfma_f32_16x16x32_bf16 v[44:47], v[92:95], v[160:163], v[44:47]\n\t" \
  "ds_read_b128 v[72:75], %[wa] offset:11264\n\t" \
  "v_mfma_f32_16x16x32_bf16 v[48:51], v[96:99], v[160:163], v[48:51]\n\t" \
  "ds_read_b128 v[76:79], %[wa] offset:12288\n\t" \
  "s_waitcnt lgkmcnt(3)\n\t" \
  "v_mfma_f32_16x16x32_bf16 v[52:55], v[100:103], v[160:163], v[52:55]\n\t" \
  "ds_read_b128 v[80:83], %[wa] offset:13312\n\t" \
  "v_mfma_f32_16x16x32_bf16 v[56:59], v[104:107], v[160:163], v[56:59]\n\t" \
  "ds_read_b128 v[84:87], %[wa] offset:14336\n\t" \
  "s_waitcnt vmcnt(3)\n\t" \
  "s_waitcnt lgkmcnt(2)\n\t" \
  "v_mfma_f32_16x16x32_bf16 v[40:43], v[68:71], v[164:167], v[40:43]\n\t" \
  "ds_read_b128 v[88:91], %[wa] offset:15360\n\t" \
  "v_mfma_f32_16x16x32_bf16 v[44:47], v[72:75], v[164:167], v[44:47]\n\t" \
  "ds_read_b128 v[92:95], %[wa] offset:16384\n\t" \
  "v_mfma_f32_16x16x32_bf16 v[48:51], v[76:79], v[164:167], v[48:51]\n\t" \
  "ds_read_b128 v[96:99], %[wa] offset:17408\n\t" \
  "s_waitcnt lgkmcnt(3)\n\t" \
  "v_mfma_f32_16x16x32_bf16 v[52:55], v[80:83], v[164:167], v[52:55]\n\t" \
  "ds_read_b128 v[100:103], %[wa] offset:18432\n\t" \
  "v_mfma_f32_16x16x32_bf16 v[56:59], v[84:87], v[164:167], v[56:59]\n\t" \
  "ds_read_b128 v[104:107], %[wa] offset:19456\n\t" \
  "s_waitcnt vmcnt(2)\n\t" \
  "s_waitcnt lgkmcnt(2)\n\t" \
  "v_mfma_f32_16x16x32_bf16 v[40:43], v[88:91], v[168:171], v[40:43]\n\t" \
  "ds_read_b128 v[68:71], %[wa] offset:20480\n\t" \
  "v_mfma_f32_16x16x32_bf16 v[44:47], v[92:95], v[168:171], v[44:47]\n\t" \
  "ds_read_b128 v[72:75], %[wa] offset:21504\n\t" \
  "v_mfma_f32_16x16x32_bf16 v[48:51], v[96:99], v[168:171], v[48:51]\n\t" \
  "ds_read_b128 v[76:79], %[wa] offset:22528\n\t" \
  "s_waitcnt lgkmcnt(3)\n\t" \
  "v_mfma_f32_16x16x32_bf16 v[52:55], v[100:103], v[168:171], v[52:55]\n\t" \
  "ds_read_b128 v[80:83], %[wa] offset:23552\n\t" \
  "v_mfma_f32_16x16x32_bf16 v[56:59], v[104:107], v[168:171], v[56:59]\n\t" \
  "ds_read_b128 v[84:87], %[wa] offset:24576\n\t" \
  "s_waitcnt vmcnt(1)\n\t" \
  "s_waitcnt lgkmcnt(2)\n\t" \
  "v_mfma_f32_16x16x32_bf16 v[40:43], v[68:71], v[172:175], v[40:43]\n\t" \
  "ds_read_b128 v[88:91], %[wa] offset:25600\n\t" \
  "v_mfma_f32_16x16x32_bf16 v[44:47], v[72:75], v[172:175], v[44:47]\n\t" \
  "ds_read_b128 v[92:95], %[wa] offset:26624\n\t" \
  "v_mfma_f32_16x16x32_bf16 v[48:51], v[76:79], v[172:175], v[48:51]\n\t" \
  "ds_read_b128 v[96:99], %[wa] offset:27648\n\t" \
  "s_waitcnt lgkmcnt(3)\n\t" \
  "v_mfma_f32_16x16x32_bf16 v[52:55], v[80:83], v[172:175], v[52:55]\n\t" \
  "ds_read_b128 v[100:103], %[wa] offset:28672\n\t" \
  "v_mfma_f32_16x16x32_bf16 v[56:59], v[84:87], v[172:175], v[56:59]\n\t" \
  "ds_read_b128 v[104:107], %[wa] offset:29696\n\t" \
  "s_waitcnt vmcnt(0)\n\t" \
  "s_waitcnt lgkmcnt(2)\n\t" \
  "v_mfma_f32_16x16x32_bf16 v[40:43], v[88:91], v[176:179], v[40:43]\n\t" \
  "v_mfma_f32_16x16x32_bf16 v[44:47], v[92:95], v[176:179], v[44:47]\n\t" \
  "v_mfma_f32_16x16x32_bf16 v[48:51], v[96:99], v[176:179], v[48:51]\n\t" \
  "s_waitcnt lgkmcnt(0)\n\t" \
  "v_mfma_f32_16x16x32_bf16 v[52:55], v[100:103], v[176:179], v[52:55]\n\t" \
  "v_mfma_f32_16x16x32_bf16 v[56:59], v[104:107], v[176:179], v[56:59]\n\t" \
  "s_nop 7\n\t" \
  "s_nop 7\n\t"
#define WO_LOOP_CLOB "v68", "v69", "v70", "v71", "v72", "v73", "v74", "v75", "v76", "v77", "v78", "v79", "v80", "v81", "v82", "v83", "v84", "v85", "v86", "v87", "v88", "v89", "v90", "v91", "v92", "v93", "v94", "v95", "v96", "v97", "v98", "v99", "v100", "v101", "v102", "v103", "v104", "v105", "v106", "v107", "v108", "v109", "v110", "v111", "v112", "v113", "v114", "v115", "v116", "v117", "v118", "v119", "v120", "v121", "v122", "v123", "v124", "v125", "v126", "v127", "v128", "v129", "v130", "v131", "v132", "v133", "v134", "v135", "v136", "v137", "v138", "v139", "v140", "v141", "v142", "v143", "v144", "v145", "v146", "v147", "v148", "v149", "v150", "v151", "v152", "v153", "v154", "v155", "v156", "v157", "v158", "v159", "v160", "v161", "v162", "v163", "v164", "v165", "v166", "v167", "v168", "v169", "v170", "v171", "v172", "v173", "v174", "v175", "v176", "v177", "v178", "v179"
#define PJ1_LOOP_ASM \
  "s_nop 4\n\t" \
  "s_mov_b32 %[so], 0\n\t" \
  "buffer_load_dwordx4 v[108:111], %[xb], %[rs], %[so] offen sc1\n\t" \
  "s_mov_b32 %[so], 1024\n\t" \
  "buffer_load_dwordx4 v[112:115], %[xb], %[rs], %[so] offen sc1\n\t" \
  "s_mov_b32 %[so], 2048\n\t" \
  "buffer_load_dwordx4 v[116:119], %[xb], %[rs], %[so] offen sc1\n\t" \
  "s_mov_b32 %[so], 3072\n\t" \
  "buffer_load_dwordx4 v[120:123], %[xb], %[rs], %[so] offen sc1\n\t" \
  "s_mov_b32 %[so], 4096\n\t" \
  "buffer_load_dwordx4 v[124:127], %[xb], %[rs], %[so] offen sc1\n\t" \
  "s_mov_b32 %[so], 5120\n\t" \
  "buffer_load_dwordx4 v[128:131], %[xb], %[rs], %[so] offen sc1\n\t" \
  "s_mov_b32 %[so], 6144\n\t" \
  "buffer_load_dwordx4 v[132:135], %[xb], %[rs], %[so] offen sc1\n\t" \
  "s_mov_b32 %[so], 7168\n\t" \
  "buffer_load_dwordx4 v[136:139], %[xb], %[rs], %[so] offen sc1\n\t" \
  "s_mov_b32 %[so], 8192\n\t" \
  "buffer_load_dwordx4 v[140:143], %[xb], %[rs], %[so] offen sc1\n\t" \
  "s_mov_b32 %[so], 9216\n\t" \
  "buffer_load_dwordx4 v[144:147], %[xb], %[rs], %[so] offen sc1\n\t" \
  "s_mov_b32 %[so], 10240\n\t" \
  "buffer_load_dwordx4 v[148:151], %[xb], %[rs], %[so] offen sc1\n\t" \
  "s_mov_b32 %[so], 11264\n\t" \
  "buffer_load_dwordx4 v[152:155], %[xb], %[rs], %[so] offen sc1\n\t" \
  "s_mov_b32 %[so], 12288\n\t" \
  "buffer_load_dwordx4 v[156:159], %[xb], %[rs], %[so] offen sc1\n\t" \
  "s_mov_b32 %[so], 13312\n\t" \
  "buffer_load_dwordx4 v[160:163], %[xb], %[rs], %[so] offen sc1\n\t" \
  "s_mov_b32 %[so], 14336\n\t" \
  "buffer_load_dwordx4 v[164:167], %[xb], %[rs], %[so] offen sc1\n\t" \
  "s_mov_b32 %[so], 15360\n\t" \
  "buffer_load_dwordx4 v[168:171], %[xb], %[rs], %[so] offen sc1\n\t" \
  "s_mov_b32 %[so], 16384\n\t" \
  "buffer_load_dwordx4 v[172:175], %[xb], %[rs], %[so] offen sc1\n\t" \
  "s_mov_b32 %[so], 17408\n\t" \
  "buffer_load_dwordx4 v[176:179], %[xb], %[rs], %[so] offen sc1\n\t" \
  "s_barrier\n\t" \
  "ds_read_b128 v[68:71], %[wa] offset:4096\n\t" \
  "ds_read_b128 v[72:75], %[wa] offset:9216\n\t" \
  "s_waitcnt lgkmcnt(1)\n\t" \
  "s_waitcnt vmcnt(17)\n\t" \
  "v_mfma_f32_16x16x32_bf16 v[40:43], v[68:71], v[108:111], 0\n\t" \
  "v_mfma_f32_16x16x32_bf16 v[52:55], v[68:71], %[one], 0\n\t" \
  "ds_read_b128 v[76:79], %[wa] offset:14336\n\t" \
  "v_mfma_f32_16x16x32_bf16 v[44:47], v[108:111], v[108:111], 0\n\t" \
  "v_mfma_f32_16x16x32_bf16 v[48:51], %[one], v[108:111], 0\n\t" \
  "s_waitcnt lgkmcnt(1)\n\t" \
  "s_waitcnt vmcnt(16)\n\t" \
  "v_mfma_f32_16x16x32_bf16 v[40:43], v[72:75], v[112:115], v[40:43]\n\t" \
  "v_mfma_f32_16x16x32_bf16 v[52:55], v[72:75], %[one], v[52:55]\n\t" \
  "ds_read_b128 v[68:71], %[wa] offset:19456\n\t" \
  "v_mfma_f32_16x16x32_bf16 v[44:47], v[112:115], v[112:115], v[44:47]\n\t" \
  "v_mfma_f32_16x16x32_bf16 v[48:51], %[one], v[112:115], v[48:51]\n\t" \
  "s_waitcnt lgkmcnt(1)\n\t" \
  "s_waitcnt vmcnt(15)\n\t" \
  "v_mfma_f32_16x16x32_bf16 v[40:43], v[76:79], v[116:119], v[40:43]\n\t" \
  "v_mfma_f32_16x16x32_bf16 v[52:55], v[76:79], %[one], v[52:55]\n\t" \
  "ds_read_b128 v[72:75], %[wa] offset:24576\n\t" \
  "v_mfma_f32_16x16x32_bf16 v[44:47], v[116:119], v[116:119], v[44:47]\n\t" \
  "v_mfma_f32_16x16x32_bf16 v[48:51], %[one], v[116:119], v[48:51]\n\t" \
  "s_waitcnt lgkmcnt(1)\n\t" \
  "s_waitcnt vmcnt(14)\n\t" \
  "v_mfma_f32_16x16x32_bf16 v[40:43], v[68:71], v[120:123], v[40:43]\n\t" \
  "v_mfma_f32_16x16x32_bf16 v[52:55], v[68:71], %[one], v[52:55]\n\t" \
  "ds_read_b128 v[76:79], %[wa] offset:29696\n\t" \
  "v_mfma_f32_16x16x32_bf16 v[44:47], v[120:123], v[120:123], v[44:47]\n\t" \
  "v_mfma_f32_16x16x32_bf16 v[48:51], %[one], v[120:123], v[48:51]\n\t" \
  "s_waitcnt lgkmcnt(1)\n\t" \
  "s_waitcnt vmcnt(13)\n\t" \
  "v_mfma_f32_16x16x32_bf16 v[40:43], v[72:75], v[124:127], v[40:43]\n\t" \
  "v_mfma_f32_16x16x32_bf16 v[52:55], v[72:75], %[one], v[52:55]\n\t" \
  "ds_read_b128 v[68:71], %[wa] offset:34816\n\t" \
  "v_mfma_f32_16x16x32_bf16 v[44:47], v[124:127], v[124:127], v[44:47]\n\t" \
  "v_mfma_f32_16x16x32_bf16 v[48:51], %[one], v[124:127], v[48:51]\n\t" \
  "s_waitcnt lgkmcnt(1)\n\t" \
  "s_waitcnt vmcnt(12)\n\t" \
  "v_mfma_f32_16x16x32_bf16 v[40:43], v[76:79], v[128:131], v[40:43]\n\t" \
  "v_mfma_f32_16x16x32_bf16 v[52:55], v[76:79], %[one], v[52:55]\n\t" \
  "ds_read_b128 v[72:75], %[wa] offset:39936\n\t" \
  "v_mfma_f32_16x16x32_bf16 v[44:47], v[128:131], v[128:131], v[44:47]\n\t" \
  "v_mfma_f32_16x16x32_bf16 v[48:51], %[one], v[128:131], v[48:51]\n\t" \
  "s_waitcnt lgkmcnt(1)\n\t" \
  "s_waitcnt vmcnt(11)\n\t" \
  "v_mfma_f32_16x16x32_bf16 v[40:43], v[68:71], v[132:135], v[40:43]\n\t" \
  "v_mfma_f32_16x16x32_bf16 v[52:55], v[68:71], %[one], v[52:55]\n\t" \
  "ds_read_b128 v[76:79], %[wa] offset:45056\n\t" \
  "v_mfma_f32_16x16x32_bf16 v[44:47], v[132:135], v[132:135], v[44:47]\n\t" \
  "v_mfma_f32_16x16x32_bf16 v[48:51], %[one], v[132:135], v[48:51]\n\t" \
  "s_waitcnt lgkmcnt(1)\n\t" \
  "s_waitcnt vmcnt(10)\n\t" \
  "v_mfma_f32_16x16x32_bf16 v[40:43], v[72:75], v[136:139], v[40:43]\n\t" \
  "v_mfma_f32_16x16x32_bf16 v[52:55], v[72:75], %[one], v[52:55]\n\t" \
  "ds_read_b128 v[68:71], %[wa] offset:50176\n\t" \
  "v_mfma_f32_16x16x32_bf16 v[44:47], v[136:139], v[136:139], v[44:47]\n\t" \
  "v_mfma_f32_16x16x32_bf16 v[48:51], %[one], v[136:139], v[48:51]\n\t" \
  "s_waitcnt lgkmcnt(1)\n\t" \
  "s_waitcnt vmcnt(9)\n\t" \
  "v_mfma_f32_16x16x32_bf16 v[40:43], v[76:79], v[140:143], v[40:43]\n\t" \
  "v_mfma_f32_16x16x32_bf16 v[52:55], v[76:79], %[one], v[52:55]\n\t" \
  "ds_read_b128 v[72:75], %[wa] offset:55296\n\t" \
  "v_mfma_f32_16x16x32_bf16 v[44:47], v[140:143], v[140:143], v[44:47]\n\t" \
  "v_mfma_f32_16x16x32_bf16 v[48:51], %[one], v[140:143], v[48:51]\n\t" \
  "s_waitcnt lgkmcnt(1)\n\t" \
  "s_waitcnt vmcnt(8)\n\t" \
  "v_mfma_f32_16x16x32_bf16 v[40:43], v[68:71], v[144:147], v[40:43]\n\t" \
  "v_mfma_f32_16x16x32_bf16 v[52:55], v[68:71], %[one], v[52:55]\n\t" \
  "ds_read_b128 v[76:79], %[wa] offset:60416\n\t" \
  "v_mfma_f32_16x16x32_bf16 v[44:47], v[144:147], v[144:147], v[44:47]\n\t" \
  "v_mfma_f32_16x16x32_bf16 v[48:51], %[one], v[144:147], v[48:51]\n\t" \
  "v_add_u32 %[wa], 61440, %[wa]\n\t" \
  "s_waitcnt lgkmcnt(1)\n\t" \
  "s_waitcnt vmcnt(7)\n\t" \
  "v_mfma_f32_16x16x32_bf16 v[40:43], v[72:75], v[148:151], v[40:43]\n\t" \
  "v_mfma_f32_16x16x32_bf16 v[52:55], v[72:75], %[one], v[52:55]\n\t" \
  "ds_read_b128 v[68:71], %[wa] offset:4096\n\t" \
  "v_mfma_f32_16x16x32_bf16 v[44:47], v[148:151], v[148:151], v[44:47]\n\t" \
  "v_mfma_f32_16x16x32_bf16 v[48:51], %[one], v[148:151], v[48:51]\n\t" \
  "s_waitcnt lgkmcnt(1)\n\t" \
  "s_waitcnt vmcnt(6)\n\t" \
  "v_mfma_f32_16x16x32_bf16 v[40:43], v[76:79], v[152:155], v[40:43]\n\t" \
  "v_mfma_f32_16x16x32_bf16 v[52:55], v[76:79], %[one], v[52:55]\n\t" \
  "ds_read_b128 v[72:75], %[wa] offset:9216\n\t" \
  "v_mfma_f32_16x16x32_bf16 v[44:47], v[152:155], v[152:155], v[44:47]\n\t" \
  "v_mfma_f32_16x16x32_bf16 v[48:51], %[one], v[152:155], v[48:51]\n\t" \
  "s_waitcnt lgkmcnt(1)\n\t" \
  "s_waitcnt vmcnt(5)\n\t" \
  "v_mfma_f32_16x16x32_bf16 v[40:43], v[68:71], v[156:159], v[40:43]\n\t" \
  "v_mfma_f32_16x16x32_bf16 v[52:55], v[68:71], %[one], v[52:55]\n\t" \
  "ds_read_b128 v[76:79], %[wa] offset:14336\n\t" \
  "v_mfma_f32_16x16x32_bf16 v[44:47], v[156:159], v[156:159], v[44:47]\n\t" \
  "v_mfma_f32_16x16x32_bf16 v[48:51], %[one], v[156:159], v[48:51]\n\t" \
  "s_waitcnt lgkmcnt(1)\n\t" \
  "s_waitcnt vmcnt(4)\n\t" \
  "v_mfma_f32_16x16x32_bf16 v[40:43], v[72:75], v[160:163], v[40:43]\n\t" \
  "v_mfma_f32_16x16x32_bf16 v[52:55], v[72:75], %[one], v[52:55]\n\t" \
  "ds_read_b128 v[68:71], %[wa] offset:19456\n\t" \
  "v_mfma_f32_16x16x32_bf16 v[44:47], v[160:163], v[160:163], v[44:47]\n\t" \
  "v_mfma_f32_16x16x32_bf16 v[48:51], %[one], v[160:163], v[48:51]\n\t" \
  "s_waitcnt lgkmcnt(1)\n\t" \
  "s_waitcnt vmcnt(3)\n\t" \
  "v_mfma_f32_16x16x32_bf16 v[40:43], v[76:79], v[164:167], v[40:43]\n\t" \
  "v_mfma_f32_16x16x32_bf16 v[52:55], v[76:79], %[one], v[52:55]\n\t" \
  "ds_read_b128 v[72:75], %[wa] offset:24576\n\t" \
  "v_mfma_f32_16x16x32_bf16 v[44:47], v[164:167], v[164:167], v[44:47]\n\t" \
  "v_mfma_f32_16x16x32_bf16 v[48:51], %[one], v[164:167], v[48:51]\n\t" \
  "s_waitcnt lgkmcnt(1)\n\t" \
  "s_waitcnt vmcnt(2)\n\t" \
  "v_mfma_f32_16x16x32_bf16 v[40:43], v[68:71], v[168:171], v[40:43]\n\t" \
  "v_mfma_f32_16x16x32_bf16 v[52:55], v[68:71], %[one], v[52:55]\n\t" \
  "ds_read_b128 v[76:79], %[wa] offset:29696\n\t" \
  "v_mfma_f32_16x16x32_bf16 v[44:47], v[168:171], v[168:171], v[44:47]\n\t" \
  "v_mfma_f32_16x16x32_bf16 v[48:51], %[one], v[168:171], v[48:51]\n\t" \
  "s_waitcnt lgkmcnt(1)\n\t" \
  "s_waitcnt vmcnt(1)\n\t" \
  "v_mfma_f32_16x16x32_bf16 v[40:43], v[72:75], v[172:175], v[40:43]\n\t" \
  "v_mfma_f32_16x16x32_bf16 v[52:55], v[72:75], %[one], v[52:55]\n\t" \
  "v_mfma_f32_16x16x32_bf16 v[44:47], v[172:175], v[172:175], v[44:47]\n\t" \
  "v_mfma_f32_16x16x32_bf16 v[48:51], %[one], v[172:175], v[48:51]\n\t" \
  "s_waitcnt lgkmcnt(0)\n\t" \
  "s_waitcnt vmcnt(0)\n\t" \
  "v_mfma_f32_16x16x32_bf16 v[40:43], v[76:79], v[176:179], v[40:43]\n\t" \
  "v_mfma_f32_16x16x32_bf16 v[52:55], v[76:79], %[one], v[52:55]\n\t" \
  "v_mfma_f32_16x16x32_bf16 v[44:47], v[176:179], v[176:179], v[44:47]\n\t" \
  "v_mfma_f32_16x16x32_bf16 v[48:51], %[one], v[176:179], v[48:51]\n\t" \
  "s_nop 7\n\t" \
  "s_nop 7\n\t"
#define PJ1_LOOP_CLOB "v68", "v69", "v70", "v71", "v72", "v73", "v74", "v75", "v76", "v77", "v78", "v79"
#define PJ2_LOOP_ASM \
  "s_nop 4\n\t" \
  "ds_read_b128 v[72:75], %[wa] offset:0\n\t" \
  "ds_read_b128 v[76:79], %[wa] offset:1024\n\t" \
  "ds_read_b128 v[80:83], %[wa] offset:2048\n\t" \
  "ds_read_b128 v[84:87], %[wa] offset:3072\n\t" \
  "s_waitcnt lgkmcnt(1)\n\t" \
  "v_mfma_f32_16x16x32_bf16 v[40:43], v[72:75], v[108:111], 0\n\t" \
  "ds_read_b128 v[88:91], %[wa] offset:5120\n\t" \
  "v_mfma_f32_16x16x32_bf16 v[44:47], v[76:79], v[108:111], 0\n\t" \
  "ds_read_b128 v[92:95], %[wa] offset:6144\n\t" \
  "v_mfma_f32_16x16x32_bf16 v[48:51], v[80:83], v[108:111], 0\n\t" \
  "ds_read_b128 v[96:99], %[wa] offset:7168\n\t" \
  "s_waitcnt lgkmcnt(3)\n\t" \
  "v_mfma_f32_16x16x32_bf16 v[52:55], v[84:87], v[108:111], 0\n\t" \
  "ds_read_b128 v[100:103], %[wa] offset:8192\n\t" \
  "s_waitcnt lgkmcnt(1)\n\t" \
  "v_mfma_f32_16x16x32_bf16 v[40:43], v[88:91], v[112:115], v[40:43]\n\t" \
  "ds_read_b128 v[72:75], %[wa] offset:10240\n\t" \
  "v_mfma_f32_16x16x32_bf16 v[44:47], v[92:95], v[112:115], v[44:47]\n\t" \
  "ds_read_b128 v[76:79], %[wa] offset:11264\n\t" \
  "v_mfma_f32_16x16x32_bf16 v[48:51], v[96:99], v[112:115], v[48:51]\n\t" \
  "ds_read_b128 v[80:83], %[wa] offset:12288\n\t" \
  "s_waitcnt lgkmcnt(3)\n\t" \
  "v_mfma_f32_16x16x32_bf16 v[52:55], v[100:103], v[112:115], v[52:55]\n\t" \
  "ds_read_b128 v[84:87], %[wa] offset:13312\n\t" \
  "s_waitcnt lgkmcnt(1)\n\t" \
  "v_mfma_f32_16x16x32_bf16 v[40:43], v[72:75], v[116:119], v[40:43]\n\t" \
  "ds_read_b128 v[88:91], %[wa] offset:15360\n\t" \
  "v_mfma_f32_16x16x32_bf16 v[44:47], v[76:79], v[116:119], v[44:47]\n\t" \
  "ds_read_b128 v[92:95], %[wa] offset:16384\n\t" \
  "v_mfma_f32_16x16x32_bf16 v[48:51], v[80:83], v[116:119], v[48:51]\n\t" \
  "ds_read_b128 v[96:99], %[wa] offset:17408\n\t" \
  "s_waitcnt lgkmcnt(3)\n\t" \
  "v_mfma_f32_16x16x32_bf16 v[52:55], v[84:87], v[116:119], v[52:55]\n\t" \
  "ds_read_b128 v[100:103], %[wa] offset:18432\n\t" \
  "s_waitcnt lgkmcnt(1)\n\t" \
  "v_mfma_f32_16x16x32_bf16 v[40:43], v[88:91], v[120:123], v[40:43]\n\t" \
  "ds_read_b128 v[72:75], %[wa] offset:20480\n\t" \
  "v_mfma_f32_16x16x32_bf16 v[44:47], v[92:95], v[120:123], v[44:47]\n\t" \
  "ds_read_b128 v[76:79], %[wa] offset:21504\n\t" \
  "v_mfma_f32_16x16x32_bf16 v[48:51], v[96:99], v[120:123], v[48:51]\n\t" \
  "ds_read_b128 v[80:83], %[wa] offset:22528\n\t" \
  "s_waitcnt lgkmcnt(3)\n\t" \
  "v_mfma_f32_16x16x32_bf16 v[52:55], v[100:103], v[120:123], v[52:55]\n\t" \
  "ds_read_b128 v[84:87], %[wa] offset:23552\n\t" \
  "s_waitcnt lgkmcnt(1)\n\t" \
  "v_mfma_f32_16x16x32_bf16 v[40:43], v[72:75], v[124:127], v[40:43]\n\t" \
  "ds_read_b128 v[88:91], %[wa] offset:25600\n\t" \
  "v_mfma_f32_16x16x32_bf16 v[44:47], v[76:79], v[124:127], v[44:47]\n\t" \
  "ds_read_b128 v[92:95], %[wa] offset:26624\n\t" \
  "v_mfma_f32_16x16x32_bf16 v[48:51], v[80:83], v[124:127], v[48:51]\n\t" \
  "ds_read_b128 v[96:99], %[wa] offset:27648\n\t" \
  "s_waitcnt lgkmcnt(3)\n\t" \
  "v_mfma_f32_16x16x32_bf16 v[52:55], v[84:87], v[124:127], v[52:55]\n\t" \
  "ds_read_b128 v[100:103], %[wa] offset:28672\n\t" \
  "s_waitcnt lgkmcnt(1)\n\t" \
  "v_mfma_f32_16x16x32_bf16 v[40:43], v[88:91], v[128:131], v[40:43]\n\t" \
  "ds_read_b128 v[72:75], %[wa] offset:30720\n\t" \
  "v_mfma_f32_16x16x32_bf16 v[44:47], v[92:95], v[128:131], v[44:47]\n\t" \
  "ds_read_b128 v[76:79], %[wa] offset:31744\n\t" \
  "v_mfma_f32_16x16x32_bf16 v[48:51], v[96:99], v[128:131], v[48:51]\n\t" \
  "ds_read_b128 v[80:83], %[wa] offset:32768\n\t" \
  "s_waitcnt lgkmcnt(3)\n\t" \
  "v_mfma_f32_16x16x32_bf16 v[52:55], v[100:103], v[128:131], v[52:55]\n\t" \
  "ds_read_b128 v[84:87], %[wa] offset:33792\n\t" \
  "s_waitcnt lgkmcnt(1)\n\t" \
  "v_mfma_f32_16x16x32_bf16 v[40:43], v[72:75], v[132:135], v[40:43]\n\t" \
  "ds_read_b128 v[88:91], %[wa] offset:35840\n\t" \
  "v_mfma_f32_16x16x32_bf16 v[44:47], v[76:79], v[132:135], v[44:47]\n\t" \
  "ds_read_b128 v[92:95], %[wa] offset:36864\n\t" \
  "v_mfma_f32_16x16x32_bf16 v[48:51], v[80:83], v[132:135], v[48:51]\n\t" \
  "ds_read_b128 v[96:99], %[wa] offset:37888\n\t" \
  "s_waitcnt lgkmcnt(3)\n\t" \
  "v_mfma_f32_16x16x32_bf16 v[52:55], v[84:87], v[132:135], v[52:55]\n\t" \
  "ds_read_b128 v[100:103], %[wa] offset:38912\n\t" \
  "s_waitcnt lgkmcnt(1)\n\t" \
  "v_mfma_f32_16x16x32_bf16 v[40:43], v[88:91], v[136:139], v[40:43]\n\t" \
  "ds_read_b128 v[72:75], %[wa] offset:40960\n\t" \
  "v_mfma_f32_16x16x32_bf16 v[44:47], v[92:95], v[136:139], v[44:47]\n\t" \
  "ds_read_b128 v[76:79], %[wa] offset:41984\n\t" \
  "v_mfma_f32_16x16x32_bf16 v[48:51], v[96:99], v[136:139], v[48:51]\n\t" \
  "ds_read_b128 v[80:83], %[wa] offset:43008\n\t" \
  "s_waitcnt lgkmcnt(3)\n\t" \
  "v_mfma_f32_16x16x32_bf16 v[52:55], v[100:103], v[136:139], v[52:55]\n\t" \
  "ds_read_b128 v[84:87], %[wa] offset:44032\n\t" \
  "s_waitcnt lgkmcnt(1)\n\t" \
  "v_mfma_f32_16x16x32_bf16 v[40:43], v[72:75], v[140:143], v[40:43]\n\t" \
  "ds_read_b128 v[88:91], %[wa] offset:46080\n\t" \
  "v_mfma_f32_16x16x32_bf16 v[44:47], v[76:79], v[140:143], v[44:47]\n\t" \
  "ds_read_b128 v[92:95], %[wa] offset:47104\n\t" \
  "v_mfma_f32_16x16x32_bf16 v[48:51], v[80:83], v[140:143], v[48:51]\n\t" \
  "ds_read_b128 v[96:99], %[wa] offset:48128\n\t" \
  "s_waitcnt lgkmcnt(3)\n\t" \
  "v_mfma_f32_16x16x32_bf16 v[52:55], v[84:87], v[140:143], v[52:55]\n\t" \
  "ds_read_b128 v[100:103], %[wa] offset:49152\n\t" \
  "s_waitcnt lgkmcnt(1)\n\t" \
  "v_mfma_f32_16x16x32_bf16 v[40:43], v[88:91], v[144:147], v[40:43]\n\t" \
  "ds_read_b128 v[72:75], %[wa] offset:51200\n\t" \
  "v_mfma_f32_16x16x32_bf16 v[44:47], v[92:95], v[144:147], v[44:47]\n\t" \
  "ds_read_b128 v[76:79], %[wa] offset:52224\n\t" \
  "v_mfma_f32_16x16x32_bf16 v[48:51], v[96:99], v[144:147], v[48:51]\n\t" \
  "ds_read_b128 v[80:83], %[wa] offset:53248\n\t" \
  "s_waitcnt lgkmcnt(3)\n\t" \
  "v_mfma_f32_16x16x32_bf16 v[52:55], v[100:103], v[144:147], v[52:55]\n\t" \
  "ds_read_b128 v[84:87], %[wa] offset:54272\n\t" \
  "s_waitcnt lgkmcnt(1)\n\t" \
  "v_mfma_f32_16x16x32_bf16 v[40:43], v[72:75], v[148:151], v[40:43]\n\t" \
  "ds_read_b128 v[88:91], %[wa] offset:56320\n\t" \
  "v_mfma_f32_16x16x32_bf16 v[44:47], v[76:79], v[148:151], v[44:47]\n\t" \
  "ds_read_b128 v[92:95], %[wa] offset:57344\n\t" \
  "v_mfma_f32_16x16x32_bf16 v[48:51], v[80:83], v[148:151], v[48:51]\n\t" \
  "ds_read_b128 v[96:99], %[wa] offset:58368\n\t" \
  "s_waitcnt lgkmcnt(3)\n\t" \
  "v_mfma_f32_16x16x32_bf16 v[52:55], v[84:87], v[148:151], v[52:55]\n\t" \
  "ds_read_b128 v[100:103], %[wa] offset:59392\n\t" \
  "v_add_u32 %[wa], 61440, %[wa]\n\t" \
  "s_waitcnt lgkmcnt(1)\n\t" \
  "v_mfma_f32_16x16x32_bf16 v[40:43], v[88:91], v[152:155], v[40:43]\n\t" \
  "ds_read_b128 v[72:75], %[wa] offset:0\n\t" \
  "v_mfma_f32_16x16x32_bf16 v[44:47], v[92:95], v[152:155], v[44:47]\n\t" \
  "ds_read_b128 v[76:79], %[wa] offset:1024\n\t" \
  "v_mfma_f32_16x16x32_bf16 v[48:51], v[96:99], v[152:155], v[48:51]\n\t" \
  "ds_read_b128 v[80:83], %[wa] offset:2048\n\t" \
  "s_waitcnt lgkmcnt(3)\n\t" \
  "v_mfma_f32_16x16x32_bf16 v[52:55], v[100:103], v[152:155], v[52:55]\n\t" \
  "ds_read_b128 v[84:87], %[wa] offset:3072\n\t" \
  "s_waitcnt lgkmcnt(1)\n\t" \
  "v_mfma_f32_16x16x32_bf16 v[40:43], v[72:75], v[156:159], v[40:43]\n\t" \
  "ds_read_b128 v[88:91], %[wa] offset:5120\n\t" \
  "v_mfma_f32_16x16x32_bf16 v[44:47], v[76:79], v[156:159], v[44:47]\n\t" \
  "ds_read_b128 v[92:95], %[wa] offset:6144\n\t" \
  "v_mfma_f32_16x16x32_bf16 v[48:51], v[80:83], v[156:159], v[48:51]\n\t" \
  "ds_read_b128 v[96:99], %[wa] offset:7168\n\t" \
  "s_waitcnt lgkmcnt(3)\n\t" \
  "v_mfma_f32_16x16x32_bf16 v[52:55], v[84:87], v[156:159], v[52:55]\n\t" \
  "ds_read_b128 v[100:103], %[wa] offset:8192\n\t" \
  "s_waitcnt lgkmcnt(1)\n\t" \
  "v_mfma_f32_16x16x32_bf16 v[40:43], v[88:91], v[160:163], v[40:43]\n\t" \
  "ds_read_b128 v[72:75], %[wa] offset:10240\n\t" \
  "v_mfma_f32_16x16x32_bf16 v[44:47], v[92:95], v[160:163], v[44:47]\n\t" \
  "ds_read_b128 v[76:79], %[wa] offset:11264\n\t" \
  "v_mfma_f32_16x16x32_bf16 v[48:51], v[96:99], v[160:163], v[48:51]\n\t" \
  "ds_read_b128 v[80:83], %[wa] offset:12288\n\t" \
  "s_waitcnt lgkmcnt(3)\n\t" \
  "v_mfma_f32_16x16x32_bf16 v[52:55], v[100:103], v[160:163], v[52:55]\n\t" \
  "ds_read_b128 v[84:87], %[wa] offset:13312\n\t" \
  "s_waitcnt lgkmcnt(1)\n\t" \
  "v_mfma_f32_16x16x32_bf16 v[40:43], v[72:75], v[164:167], v[40:43]\n\t" \
  "ds_read_b128 v[88:91], %[wa] offset:15360\n\t" \
  "v_mfma_f32_16x16x32_bf16 v[44:47], v[76:79], v[164:167], v[44:47]\n\t" \
  "ds_read_b128 v[92:95], %[wa] offset:16384\n\t" \
  "v_mfma_f32_16x16x32_bf16 v[48:51], v[80:83], v[164:167], v[48:51]\n\t" \
  "ds_read_b128 v[96:99], %[wa] offset:17408\n\t" \
  "s_waitcnt lgkmcnt(3)\n\t" \
  "v_mfma_f32_16x16x32_bf16 v[52:55], v[84:87], v[164:167], v[52:55]\n\t" \
  "ds_read_b128 v[100:103], %[wa] offset:18432\n\t" \
  "s_waitcnt lgkmcnt(1)\n\t" \
  "v_mfma_f32_16x16x32_bf16 v[40:43], v[88:91], v[168:171], v[40:43]\n\t" \
  "ds_read_b128 v[72:75], %[wa] offset:20480\n\t" \
  "v_mfma_f32_16x16x32_bf16 v[44:47], v[92:95], v[168:171], v[44:47]\n\t" \
  "ds_read_b128 v[76:79], %[wa] offset:21504\n\t" \
  "v_mfma_f32_16x16x32_bf16 v[48:51], v[96:99], v[168:171], v[48:51]\n\t" \
  "ds_read_b128 v[80:83], %[wa] offset:22528\n\t" \
  "s_waitcnt lgkmcnt(3)\n\t" \
  "v_mfma_f32_16x16x32_bf16 v[52:55], v[100:103], v[168:171], v[52:55]\n\t" \
  "ds_read_b128 v[84:87], %[wa] offset:23552\n\t" \
  "s_waitcnt lgkmcnt(1)\n\t" \
  "v_mfma_f32_16x16x32_bf16 v[40:43], v[72:75], v[172:175], v[40:43]\n\t" \
  "ds_read_b128 v[88:91], %[wa] offset:25600\n\t" \
  "v_mfma_f32_16x16x32_bf16 v[44:47], v[76:79], v[172:175], v[44:47]\n\t" \
  "ds_read_b128 v[92:95], %[wa] offset:26624\n\t" \
  "v_mfma_f32_16x16x32_bf16 v[48:51], v[80:83], v[172:175], v[48:51]\n\t" \
  "ds_read_b128 v[96:99], %[wa] offset:27648\n\t" \
  "s_waitcnt lgkmcnt(3)\n\t" \
  "v_mfma_f32_16x16x32_bf16 v[52:55], v[84:87], v[172:175], v[52:55]\n\t" \
  "ds_read_b128 v[100:103], %[wa] offset:28672\n\t" \
  "s_waitcnt lgkmcnt(1)\n\t" \
  "v_mfma_f32_16x16x32_bf16 v[40:43], v[88:91], v[176:179], v[40:43]\n\t" \
  "v_mfma_f32_16x16x32_bf16 v[44:47], v[92:95], v[176:179], v[44:47]\n\t" \
  "v_mfma_f32_16x16x32_bf16 v[48:51], v[96:99], v[176:179], v[48:51]\n\t" \
  "s_waitcnt lgkmcnt(0)\n\t" \
  "v_mfma_f32_16x16x32_bf16 v[52:55], v[100:103], v[176:179], v[52:55]\n\t" \
  "s_nop 7\n\t" \
  "s_nop 7\n\t"
#define PJ2_LOOP_CLOB "v72", "v73", "v74", "v75", "v76", "v77", "v78", "v79", "v80", "v81", "v82", "v83", "v84", "v85", "v86", "v87", "v88", "v89", "v90", "v91", "v92", "v93", "v94", "v95", "v96", "v97", "v98", "v99", "v100", "v101", "v102", "v103"

constexpr int A_HALO = TA_BYTES;
constexpr int A_TB = 0, A_TS = 304;
constexpr int A_ST = 144 * A_TS + 64;
constexpr int WO_W = 65536;
constexpr int PJ_W = 0;
constexpr int PJ_VT = TW_BYTES;
constexpr int AT_Q = KV_BYTES;
constexpr int A_CS = WO_W + TW_BYTES;
constexpr int PJ_CS = AT_Q + 9216;
constexpr int L_CNT = PJ_CS + 320;
constexpr int L_DONE = L_CNT + 4, L_RDY = L_CNT + 8, L_PUB8 = L_CNT + 12, L_PROG = L_CNT + 16, L_DONE2 = L_CNT + 20,
              L_PACE = L_CNT + 32;
constexpr int LDS_BYTES = L_CNT + 64;

struct Ctx {
  unsigned lds0, tag;
  int wave, b;
  char* ws;
};

DI void copy_image(const char* src, int from, int to, int lw, int lane, unsigned dst) {
#pragma unroll 1
  for (int ii = from + lw; ii < to; ii += 4) glds16_sc1(src, ii * 1024 + lane * 16, dst + ii * 1024);
}
DI void a_wchunk(const char* src, int ch, int lw, int lane, unsigned lds0) {
#pragma unroll
  for (int i = 0; i < 7; i++) {
    const int ii = ch * 21 + lw * 7 + i;
    glds16_sc1(src, ii * 1024 + lane * 16, lds0 + ii * 1024);
  }
}
DI void publish(const Ctx& x, unsigned* flag) {
  wait_vm<0>();
  if (get_lane() == 0) {
    const unsigned old = __hip_atomic_fetch_add((LAS unsigned*)(uintptr_t)(x.lds0 + L_CNT), 1u, __ATOMIC_RELAXED, __HIP_MEMORY_SCOPE_WORKGROUP);
    if ((old & 3u) == 3u) __hip_atomic_store((gu32*)flag, vconst(x.tag), __ATOMIC_RELAXED, __HIP_MEMORY_SCOPE_AGENT);
  }
}

DI void publish8(const Ctx& x, unsigned* flag) {
  wait_vm<0>();
  if (get_lane() == 0) {
    const unsigned old = __hip_atomic_fetch_add((LAS unsigned*)(uintptr_t)(x.lds0 + L_PUB8), 1u, __ATOMIC_RELAXED, __HIP_MEMORY_SCOPE_WORKGROUP);
    if ((old & 7u) == 7u) __hip_atomic_store((gu32*)flag, vconst(x.tag), __ATOMIC_RELAXED, __HIP_MEMORY_SCOPE_AGENT);
  }
}

DI void retile_inputs(Ctx& x, unsigned epoch, const uint16_t* x0, const uint16_t* wqkv, const uint16_t* woA,
                      const uint16_t* wq, const uint16_t* wk, const uint16_t* wv, const uint16_t* woB) {
  char* ws = x.ws;
  const int b = x.b, lane = get_lane(), wave = x.wave;
  const int mtA = (b & 7) * 2 + (b >> 7), hp = (b >> 3) & 15;
  const int mt = (b & 7) * 4 + (b >> 6), cb = (b >> 3) & 7;
  const rsrc_t rw = mk_rsrc(ws, WS_ST + ST_BYTES);
  const int row = lane >> 1, half = lane & 1;
  const int xt = 8 * mtA + (hp >> 1), xh = hp & 1;
  const char* xrow = (const char*)x0 + (xt * 16 + xh * 8) * (DM * 2) + wave * 144;
  const u32x4 xa = *(const u32x4*)(xrow + (lane >> 3) * (DM * 2) + (lane & 7) * 16);
  const u32x4 xb = *(const u32x4*)(xrow + (lane & 7) * (DM * 2) + 128);
  const int fa = mtA * 8 + wave, sa = fa / 7, ta = fa - sa * 7;
  u32x2 av[2];
#pragma unroll
  for (int i = 0; i < 2; i++) {
    const int G = 4 * ta + 2 * half + i;
    const int sg = G / 9, gi = G - sg * 9;
    const bool ok = fa < 126 && G < 27;
    av[i] = *(const u32x2*)((const char*)wqkv + (ok ? (32 * sa + row) * (3 * DM * 2) + (sg * DM + hp * 36 + 4 * gi) * 2 : 0));
  }
  u32x4 pv[2], ex[2];
  unsigned pdst[2];
  bool spare[2];
#pragma unroll
  for (int r = 0; r < 2; r++) {
    const int j = r == 0 ? wave : 8;
    const int m = j / 3, f = mt * 3 + (j - m * 3), s = f / 5, t = f - s * 5;
    const bool live = f < 90 && (r == 0 || wave == 0);
    const uint16_t* W = m == 0 ? woA : (m == 1 ? woB : wq);
    const int k = live ? 32 * s + row : 0;
    spare[r] = t == 4 && half == 1;
    pv[r] = *(const u32x4*)((const char*)W + k * (DM * 2) + (spare[r] ? 0 : cb * 144 + t * 32 + half * 16));
    ex[r] = u32x4{0u, 0u, 0u, 0u};
    if (m == 2 && t == 4) {
      const int p0 = cb <= 2 ? 4 * cb : 12 + 2 * (cb - 3), np = cb <= 2 ? 4 : (cb <= 5 ? 2 : 0);
#pragma unroll
      for (int i = 0; i < 4; i++) {
        const int P = i < np ? p0 + i : 0;
        const unsigned w = *(const unsigned*)((P < 9 ? wk + 2 * P : wv + 2 * (P - 9)) + k * 18);
        ex[r][i] = i < np ? w : 0u;
      }
    }
    pdst[r] = live ? WS_TW + (m * 8 + cb) * TW_BYTES + f * 1024 + lane * 16 : OOB;
  }
  const unsigned sc = x.lds0 + wave * 4096;
  lds_st<u32x4>(sc + 3072 + ((lane & 7) * 8 + (lane >> 3)) * 16, xa);
  lds_st<u32x4>(sc + row * 32 + half * 16, u32x4{av[0][0], av[0][1], av[1][0], av[1][1]});
#pragma unroll
  for (int r = 0; r < 2; r++) {
    u32x4 v;
#pragma unroll
    for (int i = 0; i < 4; i++) v[i] = spare[r] ? ex[r][i] : pv[r][i];
    lds_st<u32x4>(sc + 1024 * (r + 1) + row * 32 + half * 16, v);
  }
  const unsigned xdst = WS_X0 + xt * T16_TILE + (xh * 8 + (lane & 7)) * 16;
  {
    const int k8 = wave * 9 + (lane >> 3), k9 = wave * 9 + 8;
    bst128(rw, xdst + (k8 >> 2) * 1024 + (k8 & 3) * 256, lds_ld<u32x4>(sc + 3072 + lane * 16));
    bst128(rw, lane < 8 ? xdst + (k9 >> 2) * 1024 + (k9 & 3) * 256 : OOB, xb);
  }
  {
    float a1 = 0.f, a2 = 0.f, b1 = 0.f, b2 = 0.f;
#pragma unroll
    for (int i = 0; i < 4; i++) { const unsigned ua = xa[i], ub = xb[i]; acc_stats(ua, a1, a2); acc_stats(ub, b1, b2); }
    a1 = sum_8(a1) + lane_pull(b1, lane >> 3);
    a2 = sum_8(a2) + lane_pull(b2, lane >> 3);
    bst64(rw, (lane & 7) == 0 ? WS_ST + ((xt * 16 + xh * 8 + (lane >> 3)) * 8 + wave) * 8 : OOB, u32x2{__float_as_uint(a1), __float_as_uint(a2)});
  }
  const unsigned ta0 = sc + (8 * (lane >> 4) + ((lane & 15) >> 2)) * 32 + (lane & 3) * 8;
  bst128(rw, fa < 126 ? WS_TA + hp * TA_BYTES + fa * 1024 + lane * 16 : OOB, __builtin_bit_cast(u32x4, tr_frag(ta0, ta0 + 128)));
  bst128(rw, pdst[0], __builtin_bit_cast(u32x4, tr_frag(ta0 + 1024, ta0 + 1024 + 128)));
  bst128(rw, pdst[1], __builtin_bit_cast(u32x4, tr_frag(ta0 + 2048, ta0 + 2048 + 128)));
  x.tag = __builtin_amdgcn_readfirstlane(epoch) + 1u;
  wait_vm<0>();
  if (lane == 0) {
    const unsigned old = __hip_atomic_fetch_add((LAS unsigned*)(uintptr_t)(x.lds0 + L_PUB8), 1u, __ATOMIC_RELAXED, __HIP_MEMORY_SCOPE_WORKGROUP);
    if ((old & 7u) == 7u) {
      gu32* fl = (gu32*)(ws + WS_FLAGS) + 10 * 256 * FS;
      const unsigned tv = vconst(x.tag);
      __hip_atomic_store(fl + (hp * 16 + mtA) * FS, tv, __ATOMIC_RELAXED, __HIP_MEMORY_SCOPE_AGENT);
      __hip_atomic_store(fl + (256 + mtA * 16 + hp) * FS, tv, __ATOMIC_RELAXED, __HIP_MEMORY_SCOPE_AGENT);
    }
  }
}

DI void phase_a(Ctx& x, int layer) {
  char* ws = x.ws;
  unsigned lds0 = x.lds0;
  int lane = get_lane(), wave = x.wave, b = x.b;
  asm volatile("" : "+s"(wave), "+s"(b), "+s"(lds0), "+s"(ws));
  const int mt = (b & 7) * 2 + (b >> 7), hp = (b >> 3) & 15;
  const int s0 = mt * 128;
  int c = lane & 15, g = lane >> 4;
  unsigned* flags = (unsigned*)(ws + WS_FLAGS);
  const rsrc_t xr = mk_rsrc(ws + (layer == 0 ? WS_X0 : WS_X2), ACT);
  const char* wimg = ws + WS_TA + hp * TA_BYTES;
  if (wave == 0) {
    if (layer == 0) {
      const int idx = lane < 16 ? hp * 16 + lane : (lane < 32 ? 256 + mt * 16 + lane - 16 : (lane < 34 && mt > 0 ? 256 + (mt - 1) * 16 + 14 + lane - 32 : hp * 16));
      poll_one(flags + (10 * 256 + idx) * FS, x.tag);
    } else {
      poll_flags(flags + (4 * 256 + (mt > 0 ? 2 * mt - 1 : 0) * 8) * FS, mt > 0 ? 24 : 16, x.tag, lane);
    }
    __hip_atomic_store((LAS unsigned*)(uintptr_t)(lds0 + L_RDY), (unsigned)(layer * 5 + 1), __ATOMIC_RELAXED, __HIP_MEMORY_SCOPE_WORKGROUP);
  } else if (wave < 4 || wave == 7 || layer == 0) {
    wait_flags(nullptr, 0, x.tag, lane, wave, lds0 + L_RDY, layer * 5 + 1);
  }
  if (wave >= 4) {
    if (wave < 7) {
      const int lw = wave - 4;
      lane = get_lane();
      if (layer == 0) {
#pragma unroll 1
        for (int ch = 0; ch < 3; ch++) a_wchunk(wimg, ch, lw, lane, lds0);
      }
#pragma unroll 1
      for (int ch = 0; ch < 4; ch++) {
        wait_vm<14>(); bar();
        if (ch < 3) a_wchunk(wimg, ch + 3, lw, lane, lds0);
      }
      wait_vm<7>(); bar(); wait_vm<0>(); bar();
    } else {
      lane = get_lane();
      c = lane & 15;
      const unsigned hb = mt == 0 ? OOB : (mt * 8 - 1) * T16_TILE + lane * 16;
      const unsigned keep = c == 0 ? 0u : ~0u, fill = c == 0 ? 0x3F803F80u : 0u;
      u32x4 hv[18];
#pragma unroll
      for (int s = 0; s < 6; s++) hv[s] = bld128(xr, hb, s * 1024);
#pragma unroll
      for (int s = 0; s < 18; s++) {
        u32x4 f;
#pragma unroll
        for (int i = 0; i < 4; i++) f[i] = (hv[s][i] & keep) | fill;
        lds_st<u32x4>(lds0 + A_HALO + s * 1024 + lane * 16, f);
        if (s % 3 == 2) {
          bar_lgkm();
          if (s < 6) {
#pragma unroll
            for (int s2 = 6 + (s / 3) * 6; s2 < 12 + (s / 3) * 6; s2++) hv[s2] = bld128(xr, hb, s2 * 1024);
          }
        }
      }
    }
    lane = get_lane();
    if (layer == 0 && wave == 7) poll_one(flags + (10 * 256 + (hp & 7) * 16 + (lane & 15) + ((lane >> 4) & 1) * 128) * FS, x.tag);
    bar();
    lane = get_lane();
    copy_image(ws + WS_TW + ((b >> 3) & 7) * TW_BYTES, 0, 28, wave - 4, lane, lds0 + WO_W);
  } else {
    lane = get_lane();
    unsigned wa = lds0 + lane * 16;
    unsigned h0 = wa + wave * 2048, h1 = wa + (wave == 3 ? 5 : wave * 2 + 1) * 1024;
    const unsigned xb = (mt * 8 + wave * 2) * T16_TILE + lane * 16;
    const unsigned ha = lds0 + A_HALO + lane * 16;
    const rsrc_t rst = mk_rsrc(ws + WS_ST + layer * ST_BYTES, ST_BYTES);
    const u32x4 st0 = stats_load(rst, ((mt * 8 + wave * 2) * 16 + c) * 64, g);
    const u32x4 st1 = stats_load(rst, ((mt * 8 + wave * 2 + 1) * 16 + c) * 64, g);
    const u32x4 sth = stats_load(rst, mt == 0 ? OOB : ((mt * 8 - 1) * 16 + c) * 64, g);
    f32x16 o0, o1, o2, o3;
    unsigned so;
    asm volatile(A_LOOP_ASM
                 : "=&{v[40:55]}"(o0), "=&{v[56:71]}"(o1), "=&{v[72:87]}"(o2), "=&{v[88:103]}"(o3),
                   [wa] "+v"(wa), [h0] "+v"(h0), [h1] "+v"(h1), [so] "=&s"(so)
                 : [xb] "v"(xb), [ha] "v"(ha), [rs] "s"(xr)
                 : "memory", A_LOOP_CLOB);
    lane = get_lane();
    c = lane & 15; g = lane >> 4;
    f32x4 acc[2][7], hacc[2];
#pragma unroll
    for (int t = 0; t < 7; t++)
#pragma unroll
      for (int r = 0; r < 4; r++) {
        const int R0 = 4 * t + r, R1 = 28 + 4 * t + r;
        acc[0][t][r] = R0 < 16 ? o0[R0] : o1[R0 - 16];
        acc[1][t][r] = R1 < 32 ? o1[R1 - 16] : (R1 < 48 ? o2[R1 - 32] : o3[R1 - 48]);
      }
#pragma unroll
    for (int r = 0; r < 4; r++) { hacc[0][r] = o3[8 + r]; hacc[1][r] = o3[12 + r]; }
    if (c == 0) {
      lds_st<f32x4>(lds0 + A_CS + (wave * 32 + g * 4) * 4, hacc[0]);
      if (wave < 3) lds_st<f32x4>(lds0 + A_CS + (wave * 32 + 16 + g * 4) * 4, hacc[1]);
    }
    const f32x2 sv0 = stats_finish(st0), sv1 = stats_finish(st1), hst = stats_finish(sth);
    const float mean[2] = {sv0[0], sv1[0]}, rstd[2] = {sv0[1], sv1[1]};
    bar_lgkm();
    {
      const unsigned row = lds0 + A_TB + (16 + wave * 32 + c) * A_TS + 8 * g;
      unsigned qb[4];
#pragma unroll
      for (int k = 0; k < 4; k++) qb[k] = row + (k < 3 && g > k ? 12 : 0);
      const unsigned hq = (16 + wave * 32) * A_TS;
      f32x4 csv[7];
#pragma unroll
      for (int t = 0; t < 7; t++) csv[t] = lds_ld<f32x4>(lds0 + A_CS + (t * 16 + g * 4) * 4);
#pragma unroll
      for (int t = 0; t < 7; t++) {
        const float qs = t < 2 ? QSCALE : (t == 2 && g == 0 ? QSCALE : 1.0f);
        const float rs0 = rstd[0] * qs, rs1 = rstd[1] * qs;
#pragma unroll
        for (int j = 0; j < 2; j++) {
          const int c0 = 16 * t + 2 * j, grp0 = c0 / 18;
          const int thr = (18 * (grp0 + 1) - c0 + 3) / 4;
          const int k = thr > 3 ? 3 : thr - 1;
          const int oq = 2 * c0 + 12 * grp0;
          const f32x2 c2 = {csv[t][2 * j], csv[t][2 * j + 1]};
          const f32x2 y0 = (f32x2{acc[0][t][2 * j], acc[0][t][2 * j + 1]} - mean[0] * c2) * rs0;
          const f32x2 y1 = (f32x2{acc[1][t][2 * j], acc[1][t][2 * j + 1]} - mean[1] * c2) * rs1;
          if (t < 6 || g < 3) {
            lds_st<unsigned>(qb[k] + oq, pk_bf16(y0[0], y0[1]));
            lds_st<unsigned>(qb[k] + oq + 16 * A_TS, pk_bf16(y1[0], y1[1]));
            if (t >= 2 && wave == (t >> 1)) {
              const f32x2 yh = (f32x2{hacc[t & 1][2 * j], hacc[t & 1][2 * j + 1]} - hst[0] * c2) * hst[1];
              lds_st<unsigned>(qb[k] - hq + oq, pk_bf16(yh[0], yh[1]));
            }
          }
        }
      }
    }
  }
  bar_lgkm();
  lane = get_lane();
  c = lane & 15; g = lane >> 4;
  {
    const int t0 = wave * 16;
    bool ok[8];
#pragma unroll
    for (int r = 0; r < 4; r++) {
      const int dA = c + 16 - 4 * g - r, dB = c - 4 * g - r;
      ok[r] = dA <= 14 && (dA & 1) == 0 && (s0 + t0 > 0);
      ok[4 + r] = dB >= 0 && dB <= 14 && (dB & 1) == 0;
    }
    const unsigned qa = lds0 + A_TB + (16 + t0 + c) * A_TS + g * 16;
    const unsigned ka = lds0 + A_TB + (t0 + c) * A_TS + 96 + g * 16;
    const unsigned va = lds0 + A_TB + (t0 + 4 * g + (c >> 2)) * A_TS + 192 + (c & 3) * 8;
    u32x4 qf[2];
    bf16x8 kA[2], kB[2], v0[2], v1[2];
#pragma unroll
    for (int h = 0; h < 2; h++) {
      qf[h] = lds_ld<u32x4>(qa + h * 48);
      kA[h] = lds_ld<bf16x8>(ka + h * 48);
      kB[h] = lds_ld<bf16x8>(ka + h * 48 + 16 * A_TS);
      v0[h] = tr_frag(va + h * 48, va + h * 48 + 16 * A_TS);
      v1[h] = tr_frag(va + h * 48 + 32, va + h * 48 + 32 + 16 * A_TS);
    }
#pragma unroll
    for (int h = 0; h < 2; h++) {
      u32x4 q = qf[h];
      q[0] = g < 3 ? q[0] : 0u;
#pragma unroll
      for (int i = 1; i < 4; i++) q[i] = g < 2 ? q[i] : 0u;
      const f32x4 z = {0, 0, 0, 0};
      const f32x4 sA = mfma16(kA[h], as_frag(q), z), sB = mfma16(kB[h], as_frag(q), z);
      float p[8];
      float m = -3.0e38f;
#pragma unroll
      for (int r = 0; r < 8; r++) { p[r] = ok[r] ? (r < 4 ? sA[r] : sB[r - 4]) : -3.0e38f; m = fmaxf(m, p[r]); }
      m = max_x32(max_x16(m));
      float sum = 0.f;
#pragma unroll
      for (int r = 0; r < 8; r++) { p[r] = __builtin_amdgcn_exp2f(p[r] - m); sum += p[r]; }
      const float inv = __builtin_amdgcn_rcpf(sum_x32(sum_x16(sum)));
      const u32x4 pf = {pk_bf16(p[0] * inv, p[1] * inv), pk_bf16(p[2] * inv, p[3] * inv), pk_bf16(p[4] * inv, p[5] * inv), pk_bf16(p[6] * inv, p[7] * inv)};
      const f32x4 o0 = mfma16(v0[h], as_frag(pf), z), o1 = mfma16(v1[h], as_frag(pf), z);
      const unsigned sa = lds0 + A_ST + (t0 + c) * 72 + h * 36;
      lds_st<unsigned>(sa + g * 8, pk_bf16(o0[0], o0[1]));
      lds_st<unsigned>(sa + g * 8 + 4, pk_bf16(o0[2], o0[3]));
      if (g == 0) lds_st<unsigned>(sa + 32, pk_bf16(o1[0], o1[1]));
    }
    const rsrc_t orr = mk_rsrc(ws + WS_OA + layer * ACT, ACT);
    const int odd = hp & 1;
    const unsigned st = lds0 + A_ST + (t0 + c) * 72, tb = (mt * 8 + wave) * T16_TILE;
    const u32x2 lo = lds_ld<u32x2>(st + odd * 8 + g * 16), hi = lds_ld<u32x2>(st + odd * 8 + g * 16 + 8);
    const u32x2 hf = lds_ld<u32x2>(st + (odd ? 0 : 64));
    bst128(orr, tb + t16_off(36 * hp + odd * 4 + g * 8, c), u32x4{lo[0], lo[1], hi[0], hi[1]});
    bst64(orr, g == 0 ? tb + t16_off(36 * hp + (odd ? 0 : 32), c) : OOB, hf);
  }
  publish8(x, flags + ((layer * 5 + 0) * 256 + mt * 16 + hp) * FS);
  if (wave >= 4) copy_image(ws + WS_TW + ((b >> 3) & 7) * TW_BYTES, 28, 90, wave - 4, get_lane(), lds0 + WO_W);
}

DI void phase_wo(const Ctx& x, int layer, int kind, uint16_t* out) {
  char* ws = x.ws;
  unsigned lds0 = x.lds0;
  int lane = get_lane(), wave = x.wave, b = x.b;
  asm volatile("" : "+s"(wave), "+s"(b), "+s"(lds0), "+s"(ws));
  const int mt = (b & 7) * 4 + (b >> 6), cb = (b >> 3) & 7;
  unsigned* flags = (unsigned*)(ws + WS_FLAGS);
  const int ph = layer * 5 + (kind == 0 ? 1 : 4);
  if (wave >= 4) {
    lane = get_lane();
    wait_vm<0>();
    bar();
    if (kind == 0) copy_image(ws + WS_TW + (16 + cb) * TW_BYTES, 0, 64, wave - 4, lane, lds0 + PJ_W);
    else if (layer == 0 && wave < 7) {
#pragma unroll 1
      for (int ch = 0; ch < 3; ch++) a_wchunk(ws + WS_TA + ((b >> 3) & 15) * TA_BYTES, ch, wave - 4, lane, lds0);
    }
    bar();
    if (kind == 0) copy_image(ws + WS_TW + (16 + cb) * TW_BYTES, 64, 90, wave - 4, lane, lds0 + PJ_W);
    return;
  }
  int c = lane & 15, g = lane >> 4;
  const bool out_nat = (layer == 1 && kind == 1);
  const unsigned xin_off = kind == 0 ? (layer == 0 ? WS_X0 : WS_X2) : (layer == 0 ? WS_X1 : WS_X3);
  const unsigned xout_off = kind == 0 ? (layer == 0 ? WS_X1 : WS_X3) : WS_X2;
  const rsrc_t rin = mk_rsrc(ws + xin_off, ACT);
  const rsrc_t ro = mk_rsrc(ws + (kind == 0 ? WS_OA : WS_OB) + layer * ACT, ACT);
  const int T = mt * 4 + wave;
  u32x2 res[5];
#pragma unroll
  for (int t = 0; t < 5; t++) {
    const int n0 = cb * 72 + t * 16 + g * 4;
    res[t] = bld64(rin, (t < 4 || g < 2) ? T * T16_TILE + t16_off(n0, c) : OOB);
  }
  if (kind == 0) wait_flags(flags + ((layer * 5 + 0) * 256 + (mt >> 1) * 16) * FS, 16, x.tag, lane, wave, lds0 + L_RDY, ph + 1);
  else wait_flags(flags + ((layer * 5 + 3) * 256 + mt * 8) * FS, 8, x.tag, lane, wave, lds0 + L_RDY, ph + 1);
  f32x16 o0;
  f32x4 o1;
  {
    unsigned wa = lds0 + WO_W + lane * 16, so;
    const unsigned xb = T * T16_TILE + lane * 16;
    asm volatile(WO_LOOP_ASM
                 : "=&{v[40:55]}"(o0), "=&{v[56:59]}"(o1), [wa] "+v"(wa), [so] "=&s"(so)
                 : [xb] "v"(xb), [rs] "s"(ro)
                 : "memory", WO_LOOP_CLOB);
  }
  lane = get_lane();
  c = lane & 15; g = lane >> 4;
  u32x2 r[6];
#pragma unroll
  for (int t = 0; t < 5; t++) {
    float a[4];
#pragma unroll
    for (int i = 0; i < 4; i++) a[i] = t < 4 ? o0[4 * t + i] : o1[i];
    r[t][0] = pk_bf16(a[0] + bf_lo(res[t][0]), a[1] + bf_hi(res[t][0]));
    r[t][1] = pk_bf16(a[2] + bf_lo(res[t][1]), a[3] + bf_hi(res[t][1]));
  }
  if (kind == 1 && layer == 0) {
    float s1 = 0.f, s2 = 0.f;
#pragma unroll
    for (int t = 0; t < 5; t++) {
      const unsigned u0 = (t < 4 || g < 2) ? r[t][0] : 0u, u1 = (t < 4 || g < 2) ? r[t][1] : 0u;
      acc_stats(u0, s1, s2); acc_stats(u1, s1, s2);
    }
    s1 = sum_x32(sum_x16(s1)); s2 = sum_x32(sum_x16(s2));
    bst64(mk_rsrc(ws + WS_ST + ST_BYTES, ST_BYTES), g == 0 ? ((T * 16 + c) * 8 + cb) * 8 : OOB, u32x2{__float_as_uint(s1), __float_as_uint(s2)});
  }
  r[5] = r[4];
  const rsrc_t rout = mk_rsrc(out_nat ? (void*)out : (void*)(ws + xout_off), ACT);
#pragma unroll
  for (int u = 0; u < 3; u++) {
    const unsigned a0 = r[2 * u][0], a1 = r[2 * u][1], b0 = r[2 * u + 1][0], b1 = r[2 * u + 1][1];
    const auto s0 = __builtin_amdgcn_permlane16_swap(a0, b0, false, false);
    const auto s1 = __builtin_amdgcn_permlane16_swap(a1, b1, false, false);
    const int n0 = cb * 72 + (2 * u + (g & 1)) * 16 + (g >> 1) * 8;
    const bool ok = u < 2 || g == 0;
    const unsigned off = out_nat ? (T * 16 + c) * (DM * 2) + n0 * 2 : T * T16_TILE + t16_off(n0, c);
    bst128(rout, ok ? off : OOB, u32x4{s0[0], s1[0], s0[1], s1[1]});
  }
  bar();
  if (!out_nat) publish(x, flags + (ph * 256 + mt * 8 + cb) * FS);
}

DI void phase_proj(const Ctx& x, int layer) {
  char* ws = x.ws;
  unsigned lds0 = x.lds0;
  int lane = get_lane(), wave = x.wave, b = x.b;
  asm volatile("" : "+s"(wave), "+s"(b), "+s"(lds0), "+s"(ws));
  const int mt = (b & 7) * 4 + (b >> 6), cb = (b >> 3) & 7;
  unsigned* flags = (unsigned*)(ws + WS_FLAGS);
  const int ph = layer * 5 + 2;
  if (wave >= 4) {
    wait_vm<0>();
    bar();
    if (layer == 0) {
      const int c = lane & 15, g = lane >> 4, t = wave - 4;
      const bf16x8 ones = as_frag(ones4());
      const unsigned at = lds0 + PJ_W + t * 1024 + lane * 16;
      f32x4 s0 = {0, 0, 0, 0}, s1 = {0, 0, 0, 0};
      bf16x8 w0[18];
#pragma unroll
      for (int i = 0; i < 18; i++) w0[i] = lds_ld<bf16x8>(at + i * 5120);
      SB();
#pragma unroll
      for (int i = 0; i < 9; i++) { s0 = mfma16(w0[2 * i], ones, s0); s1 = mfma16(w0[2 * i + 1], ones, s1); }
      if (c == 0) lds_st<f32x4>(lds0 + PJ_CS + (t * 16 + g * 4) * 4, s0 + s1);
      asm volatile("s_waitcnt lgkmcnt(0)" ::: "memory");
      if (lane == 0) __hip_atomic_fetch_add((LAS unsigned*)(uintptr_t)(lds0 + L_DONE2), 1u, __ATOMIC_RELAXED, __HIP_MEMORY_SCOPE_WORKGROUP);
    }
    return;
  }
  wait_flags(flags + ((layer * 5 + 1) * 256 + mt * 8) * FS, 8, x.tag, lane, wave, lds0 + L_RDY, ph + 1);
  const rsrc_t rx = mk_rsrc(ws + (layer == 0 ? WS_X1 : WS_X3), ACT);
  const int T = mt * 4 + wave;
  f32x16 o0, x0, x1, x2, x3;
  f32x8 x4;
  {
    unsigned wa = lds0 + PJ_W + lane * 16, so;
    const unsigned xb = T * T16_TILE + lane * 16;
    const u32x4 one = ones4();
    asm volatile(PJ1_LOOP_ASM
                 : "=&{v[40:55]}"(o0), "=&{v[108:123]}"(x0), "=&{v[124:139]}"(x1), "=&{v[140:155]}"(x2), "=&{v[156:171]}"(x3),
                   "=&{v[172:179]}"(x4), [wa] "+v"(wa), [so] "=&s"(so)
                 : [xb] "v"(xb), [rs] "s"(rx), [one] "v"(one)
                 : "memory", PJ1_LOOP_CLOB);
  }
  lane = get_lane();
  int c = lane & 15, g = lane >> 4;
  const float mean = o0[8] * (1.0f / DM);
  const float rstd = __builtin_amdgcn_rsqf(fmaxf(gram_diag(f32x4{o0[4], o0[5], o0[6], o0[7]}, lane) * (1.0f / DM) - mean * mean, 0.f) + LN_EPS);
  const rsrc_t rkv = mk_rsrc(ws + WS_KV + layer * 262144, KV_BYTES);
  f32x4 y4;
#pragma unroll
  for (int r = 0; r < 4; r++) y4[r] = (o0[r] - mean * o0[12 + r]) * rstd;
  {
    const unsigned u0 = pk_bf16(y4[0], y4[1]), u1 = pk_bf16(y4[2], y4[3]);
    const int tile = T >> 1, r32 = (T & 1) * 16 + c;
    const unsigned tb = tile * KV_TILE, vb = tb + KV_V + (T & 1) * KV_VG;
    if (cb < 2) {
      const auto s0 = __builtin_amdgcn_permlane16_swap(u0, u0, false, false);
      const auto s1 = __builtin_amdgcn_permlane16_swap(u1, u1, false, false);
      bst128(rkv, g == 2 ? tb + cb * 512 + r32 * 16 : OOB, u32x4{s0[0], s1[0], s0[1], s1[1]});
    } else if (cb < 6) {
      const int nk = cb == 2 ? 2 : 0;
      const int d0 = cb == 2 ? 0 : 6 + 4 * (cb - 3);
      const int nv = cb == 2 ? 6 : 4;
      const unsigned sc = lds0 + PJ_VT + wave * 256;
      const unsigned pos = ((c >> 2) & 1) * 16 + ((c & 3) + 4 * (c >> 3)) * 2;
      if (g >= 2) {
        const int col = (g - 2) * 4;
        lds_st<unsigned short>(sc + (col + 0) * 32 + pos, (unsigned short)u0);
        lds_st<unsigned short>(sc + (col + 1) * 32 + pos, (unsigned short)(u0 >> 16));
        lds_st<unsigned short>(sc + (col + 2) * 32 + pos, (unsigned short)u1);
        lds_st<unsigned short>(sc + (col + 3) * 32 + pos, (unsigned short)(u1 >> 16));
      }
      bst32(rkv, (nk && g == 2) ? tb + KV_KX + r32 * 4 : OOB, u0);
      const int dv = c >> 1, hh = c & 1;
      const u32x4 pv = lds_ld<u32x4>(sc + ((dv + nk) & 7) * 32 + hh * 16);
      bst128(rkv, (g == 0 && dv < nv) ? vb + hh * KV_VH + (d0 + dv) * 16 : OOB, pv);
    } else if (cb == 7) {
      bst128(rkv, lane < 2 ? vb + lane * KV_VH + 18 * 16 : OOB, ones4());
    }
  }
  publish(x, flags + (ph * 256 + mt * 8 + cb) * FS);
  const unsigned qrow = lds0 + AT_Q + (wave * 16 + c) * 144 + g * 8;
  if (g < 2) lds_st<u32x2>(qrow + 128, u32x2{pk_bf16(y4[0] * QSCALE, y4[1] * QSCALE), pk_bf16(y4[2] * QSCALE, y4[3] * QSCALE)});
  f32x16 q0;
  {
    unsigned wa = lds0 + PJ_W + lane * 16;
    asm volatile(PJ2_LOOP_ASM
                 : "=&{v[40:55]}"(q0), [wa] "+v"(wa)
                 : "{v[108:123]}"(x0), "{v[124:139]}"(x1), "{v[140:155]}"(x2), "{v[156:171]}"(x3), "{v[172:179]}"(x4)
                 : "memory", PJ2_LOOP_CLOB);
  }
#pragma unroll 1
  for (unsigned spin = 0; spin < (SPIN_CAP << 4); spin++) {
    if (__hip_atomic_load((LAS unsigned*)(uintptr_t)(lds0 + L_DONE2), __ATOMIC_RELAXED, __HIP_MEMORY_SCOPE_WORKGROUP) >= 4u) break;
    __builtin_amdgcn_s_sleep(1);
  }
  asm volatile("" ::: "memory");
  {
    const float rq = rstd * QSCALE;
#pragma unroll
    for (int t = 0; t < 4; t++) {
      const f32x4 cs = lds_ld<f32x4>(lds0 + PJ_CS + (t * 16 + g * 4) * 4);
      f32x4 y;
#pragma unroll
      for (int r = 0; r < 4; r++) y[r] = (q0[4 * t + r] - mean * cs[r]) * rq;
      lds_st<u32x2>(qrow + t * 32, u32x2{pk_bf16(y[0], y[1]), pk_bf16(y[2], y[3])});
    }
  }
}

DI void phase_att(const Ctx& x, int layer) {
  char* ws = x.ws;
  unsigned lds0 = x.lds0;
  int lane = get_lane(), wave = x.wave, b = x.b;
  asm volatile("" : "+s"(wave), "+s"(b), "+s"(lds0), "+s"(ws));
  const int mt = (b & 7) * 4 + (b >> 6), cb = (b >> 3) & 7;
  unsigned* flags = (unsigned*)(ws + WS_FLAGS);
  const int ph = layer * 5 + 3;
  if (wave == 4) {
#pragma unroll 1
    for (unsigned spin = 0; spin < (SPIN_CAP << 4); spin++) {
      if (__hip_atomic_load((LAS unsigned*)(uintptr_t)(lds0 + L_CNT), __ATOMIC_RELAXED, __HIP_MEMORY_SCOPE_WORKGROUP) >= 12u * layer + 8u) break;
      __builtin_amdgcn_s_sleep(1);
    }
    gu32* f = (gu32*)flags + ((layer * 5 + 2) * 256 + lane) * FS;
    unsigned seen = 0;
#pragma unroll 1
    for (unsigned spin = 0; spin < SPIN_CAP; spin++) {
      unsigned v[4];
#pragma unroll
      for (int j = 0; j < 4; j++)
        v[j] = ((seen >> j) & 1) ? x.tag : __hip_atomic_load(f + j * 64 * FS, __ATOMIC_RELAXED, __HIP_MEMORY_SCOPE_AGENT);
#pragma unroll
      for (int j = 0; j < 4; j++) seen |= (v[j] == x.tag ? 1u : 0u) << j;
      if (__ballot(seen != 15u) == 0ull) break;
      __builtin_amdgcn_s_sleep(POLL_SLEEP);
    }
    asm volatile("" ::: "memory");
    if (layer == 0 && b == 0 && lane == 0)
      __hip_atomic_store((gu32*)(ws + WS_EPOCH), vconst(x.tag), __ATOMIC_RELAXED, __HIP_MEMORY_SCOPE_AGENT);
  }
  bar_lgkm();
  const char* kv = ws + WS_KV + layer * 262144;
#pragma unroll 1
  for (int ii = wave; ii < 43; ii += 8) glds16_sc1(kv, ii * 1024 + lane * 16, lds0 + ii * 1024);
  const int r = lane & 31, h = lane >> 5;
  const unsigned qa = lds0 + AT_Q + (wave * 8 + (r >> 2)) * 144 + (r & 3) * 36;
  u32x4 qw;
#pragma unroll
  for (int j = 0; j < 4; j++) qw[j] = lds_ld<unsigned>(qa + h * 16 + j * 4);
  const bf16x8 qf = as_frag(qw);
  const u32x2 qx = {h == 0 ? lds_ld<unsigned>(qa + 32) : 0u, 0u};
  const unsigned ak = lds0 + h * 512 + r * 16, akx = lds0 + KV_KX + r * 4;
  const unsigned av = lds0 + KV_V + h * KV_VH + (r < 18 ? r : 18) * 16;
  wait_vm<0>();
  bar_lgkm();
  if (wave >= 4) {
#pragma unroll 1
    for (int ii = 43 + wave - 4; ii < 148; ii += 4) glds16_sc1(kv, ii * 1024 + lane * 16, lds0 + ii * 1024);
    wait_vm<0>();
    if (lane == 0) __hip_atomic_fetch_add((LAS unsigned*)(uintptr_t)(lds0 + L_PROG), 1u, __ATOMIC_RELAXED, __HIP_MEMORY_SCOPE_WORKGROUP);
  }
  f32x16 O = {0, 0, 0, 0, 0, 0, 0, 0, 0, 0, 0, 0, 0, 0, 0, 0};
  {
    unsigned ka = ak, kxa = akx, va = av, cnt, sc;
    const unsigned ca = vconst(lds0 + L_PROG), tg = 4u * (layer + 1);
    const unsigned pa = vconst(lds0 + L_PACE + wave * 4), pb = vconst(lds0 + L_PACE + (wave ^ 4) * 4);
    lds_st<unsigned>(pa, 8u);
    if (wave >= 4) __builtin_amdgcn_s_setprio(1);
    asm volatile(ATTN_LOOP_ASM
                 : [o] "+v"(O), [ka] "+v"(ka), [kxa] "+v"(kxa), [va] "+v"(va), [cnt] "=&s"(cnt), [sc] "=&s"(sc)
                 : [qf] "v"(qf), [qx] "v"(qx), [ca] "v"(ca), [tg] "s"(tg), [pa] "v"(pa), [pb] "v"(pb)
                 : "memory", "scc", ATTN_LOOP_CLOB);
  }
  float l = h == 0 ? O[10] : O[8];
  {
    float mx = 0.f;
#pragma unroll
    for (int j = 0; j < 10; j++) mx = fmaxf(mx, fabsf(O[j]));
    bool ok = (l >= 8.2718061e-25f) && (l <= 1.2676506e30f) && (mx <= 3.0e38f);
#pragma unroll
    for (int j = 0; j < 10; j++) ok = ok && (O[j] == O[j]);
    if (__ballot(!ok) != 0ull) {
      const f32x16 zero = {0, 0, 0, 0, 0, 0, 0, 0, 0, 0, 0, 0, 0, 0, 0, 0};
      float m = -3.0e38f;
#pragma unroll 1
      for (int i = 0; i < 64; i++) {
        const bf16x8 kf = lds_ld<bf16x8>(ak + i * KV_TILE);
        const u32x2 kx = {lds_ld<unsigned>(akx + i * KV_TILE), 0u};
        f32x16 s = mfma32x8(kx, qx, mfma32(kf, qf, zero));
#pragma unroll
        for (int j = 0; j < 16; j++) m = fmaxf(m, s[j]);
      }
      m = max_x32(m);
      f32x16 negm;
#pragma unroll
      for (int j = 0; j < 16; j++) { negm[j] = -m; O[j] = 0.f; }
#pragma unroll 1
      for (int i = 0; i < 64; i++) {
        const unsigned off = i * KV_TILE;
        const bf16x8 kf = lds_ld<bf16x8>(ak + off);
        const u32x2 kx = {lds_ld<unsigned>(akx + off), 0u};
        const bf16x8 v0 = lds_ld<bf16x8>(av + off), v1 = lds_ld<bf16x8>(av + off + KV_VG);
        const f32x16 s = mfma32x8(kx, qx, mfma32(kf, qf, negm));
        u32x4 p0, p1;
#pragma unroll
        for (int j = 0; j < 4; j++) {
          p0[j] = pk_bf16(__builtin_amdgcn_exp2f(s[2 * j]), __builtin_amdgcn_exp2f(s[2 * j + 1]));
          p1[j] = pk_bf16(__builtin_amdgcn_exp2f(s[8 + 2 * j]), __builtin_amdgcn_exp2f(s[8 + 2 * j + 1]));
        }
        O = mfma32(v0, as_frag(p0), O);
        O = mfma32(v1, as_frag(p1), O);
      }
      l = h == 0 ? O[10] : O[8];
    }
  }
  lane = get_lane();
  LAS unsigned* done = (LAS unsigned*)(uintptr_t)(lds0 + L_DONE);
  {
    const int r2 = lane & 31, h2 = lane >> 5;
    const float inv = 1.0f / l;
    const unsigned st = lds0 + AT_Q + wave * 1152;
    const unsigned sa = st + (r2 >> 2) * 144 + (r2 & 3) * 36 + h2 * 8;
    lds_st<unsigned>(sa, pk_bf16(O[0] * inv, O[1] * inv));
    lds_st<unsigned>(sa + 4, pk_bf16(O[2] * inv, O[3] * inv));
    lds_st<unsigned>(sa + 16, pk_bf16(O[4] * inv, O[5] * inv));
    lds_st<unsigned>(sa + 20, pk_bf16(O[6] * inv, O[7] * inv));
    if (h2 == 0) lds_st<unsigned>(sa + 32, pk_bf16(O[8] * inv, O[9] * inv));
    const rsrc_t orr = mk_rsrc(ws + WS_OB + layer * ACT, ACT);
    const unsigned tb = (mt * 4 + (wave >> 1)) * T16_TILE + ((wave & 1) * 8 + (lane & 7)) * 16;
#pragma unroll
    for (int it = 0; it < 2; it++) {
      const int gi = it * 8 + (lane >> 3);
      const u32x4 v = lds_ld<u32x4>(st + (lane & 7) * 144 + (gi < 9 ? gi : 0) * 16);
      const int k8 = cb * 9 + gi;
      bst128(orr, gi < 9 ? tb + (k8 >> 2) * 1024 + (k8 & 3) * 256 : OOB, v);
    }
  }
  asm volatile("s_waitcnt lgkmcnt(0)" ::: "memory");
  if (lane == 0) __hip_atomic_fetch_add(done, 1u, __ATOMIC_RELAXED, __HIP_MEMORY_SCOPE_WORKGROUP);
  publish8(x, flags + (ph * 256 + mt * 8 + cb) * FS);
  if (wave >= 1 && wave < 4) {
#pragma unroll 1
    for (unsigned spin = 0; spin < (SPIN_CAP << 4); spin++) {
      if (__hip_atomic_load(done, __ATOMIC_RELAXED, __HIP_MEMORY_SCOPE_WORKGROUP) >= 8u * (layer + 1)) break;
      __builtin_amdgcn_s_sleep(1);
    }
    asm volatile("" ::: "memory");
    const int l2 = get_lane();
#pragma unroll 1
    for (int ii = wave - 1; ii < 90; ii += 3) glds16_sc1(ws + WS_TW + (8 + cb) * TW_BYTES, ii * 1024 + l2 * 16, lds0 + WO_W + ii * 1024);
  }
}

__global__ void __launch_bounds__(512, 2) fused_net(const uint16_t* __restrict__ x0, const uint16_t* __restrict__ wqkv,
                                                 const uint16_t* __restrict__ woA, const uint16_t* __restrict__ wq,
                                                 const uint16_t* __restrict__ wk, const uint16_t* __restrict__ wv,
                                                 const uint16_t* __restrict__ woB, uint16_t* __restrict__ out,
                                                 char* __restrict__ ws) {
  Ctx x;
  x.lds0 = lds_addr(dyn_smem);
  x.wave = __builtin_amdgcn_readfirstlane(threadIdx.x >> 6);
  x.b = blockIdx.x;
  x.ws = ws;
  x.tag = 0;
  const unsigned epoch = __hip_atomic_load((gu32*)(ws + WS_EPOCH), __ATOMIC_RELAXED, __HIP_MEMORY_SCOPE_AGENT);
  if (threadIdx.x < 12) lds_st<unsigned>(x.lds0 + L_CNT + threadIdx.x * 4, 0u);
  bar_lgkm();
  retile_inputs(x, epoch, x0, wqkv, woA, wq, wk, wv, woB);
#pragma unroll 1
  for (int it = 0; it < 4; it++) {
    const int layer = it >> 1, kind = it & 1;
    if (kind == 0) {
      phase_a(x, layer);
    } else {
      phase_proj(x, layer);
      phase_att(x, layer);
    }
    phase_wo(x, layer, kind, out);
  }
}

extern "C" void kernel_launch(void* const* d_in, const int* in_sizes, int n_in, void* d_out, int out_size,
                              void* d_ws, size_t ws_size, hipStream_t stream) {
  (void)hipFuncSetAttribute((const void*)fused_net, hipFuncAttributeMaxDynamicSharedMemorySize, LDS_BYTES);
  fused_net<<<256, 512, LDS_BYTES, stream>>>((const uint16_t*)d_in[0], (const uint16_t*)d_in[1], (const uint16_t*)d_in[2],
                                             (const uint16_t*)d_in[4], (const uint16_t*)d_in[5], (const uint16_t*)d_in[6],
                                             (const uint16_t*)d_in[7], (uint16_t*)d_out, (char*)d_ws);
}
```
